```python
import torch

def _softmax(x, axis=-1):
    return torch.softmax(x, dim=axis)
def _gelu(x):
    return torch.nn.functional.gelu(x, approximate="tanh")
def _silu(x):
    return torch.nn.functional.silu(x)
def _sigmoid(x):
    return torch.sigmoid(x)
def _layernorm(x, axis=-1, eps=1e-5):
    m = x.mean(dim=axis, keepdim=True)
    v = x.var(dim=axis, keepdim=True, unbiased=False)
    return (x - m) / torch.sqrt(v + eps)
def _scatter_add(tgt, idx, src, axis=0):
    return tgt.clone().index_add_(axis, idx, src)
def _segment_sum(src, seg, n):
    out = torch.zeros((n,) + src.shape[1:], dtype=src.dtype)
    return out.index_add_(0, seg, src)
def _pad(x, pw):
    flat = tuple(v for pair in reversed(pw) for v in pair)
    return torch.nn.functional.pad(x, flat)

kernel_name = 'standalone__spectral_fnet__prompt'

def setup_inputs():

    _w = (lambda a: (lambda t: t.to(torch.bfloat16) if t.is_floating_point() else t)(torch.as_tensor(a)))
    B, S, D = 1, 2048, 448
    ns = torch.arange(S)[:, None] * torch.arange(S)[None, :]
    nd = torch.arange(D)[:, None] * torch.arange(D)[None, :]
    Ws_re = torch.cos(-2.0 * torch.pi * ns / S).to(float)
    Ws_im = torch.sin(-2.0 * torch.pi * ns / S).to(float)
    Wd_re = torch.cos(-2.0 * torch.pi * nd / D).to(float)
    Wd_im = torch.sin(-2.0 * torch.pi * nd / D).to(float)
    return {
        "x": _w((lambda *s: torch.randn(*s, dtype=torch.bfloat16) * 0.05)(B, S, D)),
        "dft_seq_re": _w(Ws_re),
        "dft_seq_im": _w(Ws_im),
        "dft_hid_re": _w(Wd_re),
        "dft_hid_im": _w(Wd_im),
    }

def reference(x, dft_seq_re, dft_seq_im, dft_hid_re, dft_hid_im):
    a = torch.einsum("bsd,ed->bse", x, dft_hid_re)
    b = torch.einsum("bsd,ed->bse", x, dft_hid_im)
    y = torch.einsum("ts,bse->bte", dft_seq_re, a) - torch.einsum("ts,bse->bte", dft_seq_im, b)
    return y
```

```cpp
#define P_TLBPF 0
#include <hip/hip_runtime.h>
#include <hip/hip_bf16.h>
#include <stdint.h>
typedef __hip_bfloat16 bf16;
typedef __attribute__((ext_vector_type(8))) short bf16x8;
typedef __attribute__((ext_vector_type(4))) short s16x4;
typedef __attribute__((ext_vector_type(4))) float f32x4;
typedef __attribute__((ext_vector_type(16))) float f32x16;
typedef __attribute__((ext_vector_type(2))) unsigned u32x2;
typedef __attribute__((ext_vector_type(4))) unsigned u32x4;
typedef __attribute__((ext_vector_type(2))) float f32x2;
typedef __attribute__((ext_vector_type(2))) __bf16 bf16x2_t;
typedef __attribute__((address_space(3))) s16x4 lds_s16x4;
typedef __attribute__((address_space(3))) bf16x8 lds_bf16x8;
typedef __attribute__((address_space(3))) f32x4 lds_f32x4;
typedef __attribute__((address_space(3))) unsigned lds_u32;
typedef __attribute__((address_space(1))) unsigned long long gu64;
#define RLX_AGENT __ATOMIC_RELAXED, __HIP_MEMORY_SCOPE_AGENT

constexpr int S = 2048, D = 448;
#define WAIT_V(n) asm volatile("s_waitcnt vmcnt(%0)" ::"n"(n) : "memory")
#define WAIT_L(n) asm volatile("s_waitcnt lgkmcnt(%0)" ::"n"(n) : "memory")
#define SCHED0() __builtin_amdgcn_sched_barrier(0)
#define BAR() __builtin_amdgcn_s_barrier()

#define TS_BEGIN(k)
#define TS_END(k)
#define TS_MARK(k, cond)
#define TS_ARG
#define TS_PASS
#define PR_ARGS
#define PR_PASS
#define PR_MARK(k)
#define PR_KT 99
#define PROBE_TAIL
#define P_AUXW 0
#define P_ROT 1
#define P_F 2
#define P_START 1
#define P_XPF 1
#define P_WDPF 3
#define P_GROUPW 0
#define P_YAUX 0
#define P_CODEPF 1

__device__ __forceinline__ unsigned pack2(float a, float b) {
  bf16x2_t v = __builtin_convertvector(f32x2{a, b}, bf16x2_t);
  return __builtin_bit_cast(unsigned, v);
}

template <int AUX>
__device__ __forceinline__ void dma16(__amdgpu_buffer_rsrc_t rs, unsigned voff, unsigned soff, unsigned lds) {
  if constexpr (AUX == 2)
    asm volatile("s_mov_b32 m0, %0\n\ts_nop 0\n\tbuffer_load_dwordx4 %1, %2, %3 offen nt lds" ::"s"(lds), "v"(voff), "s"(rs), "s"(soff) : "memory");
  else
    asm volatile("s_mov_b32 m0, %0\n\ts_nop 0\n\tbuffer_load_dwordx4 %1, %2, %3 offen lds" ::"s"(lds), "v"(voff), "s"(rs), "s"(soff) : "memory");
}

constexpr int KS = 2, KLEN = S / KS, NK = KLEN / 64;
constexpr int A_B = 128 * 128, STAGE_B = 32768;
constexpr int LDS_BYTES = 160 * 1024;
constexpr int PART_BYTES = KS * 256 * 14 * 1024;
constexpr int SYNC_OFF = 16 << 20;
constexpr int NST = 5;
__host__ __device__ constexpr int unit_slot(int u) { return (0x2420131 >> (4 * u)) & 7; }
constexpr int RED_OFF = 3 * STAGE_B, RED_LD = 464;
#define P_PB0 3
#define P_PBS 2
constexpr int CNT_OFF = 3 * STAGE_B + 15360;
#define CODE_PF_BYTES 11520

__device__ __forceinline__ void rdD(bf16x8* d, unsigned dbase, int hi, int n, int slot_lo, int slot_hi) {
  const unsigned eb = (dbase ^ ((n & 1) << 6)) + (hi ? slot_hi : slot_lo) * STAGE_B;
#pragma unroll
  for (int c = 0; c < 7; ++c) d[c] = *(const lds_bf16x8*)(size_t)(eb + c * 2048);
}

template <int NJ>
__device__ __forceinline__ void p1_compute(unsigned lds0, int wid, int lane, int kb0, unsigned poff, __amdgpu_buffer_rsrc_t rsP,
                                           __amdgpu_buffer_rsrc_t rsPf, unsigned pfo, bf16x8 (&de)[4][7], unsigned dbase PR_ARGS) {
  f32x16 acc[2][NJ];
#pragma unroll
  for (int i = 0; i < 2; ++i)
#pragma unroll
    for (int j = 0; j < NJ; ++j)
#pragma unroll
      for (int r = 0; r < 16; ++r) acc[i][j][r] = 0.f;
  const int wm = wid >> 1, wn = wid & 1;
  const int h = lane >> 5, r32 = lane & 31;
  const unsigned abase = lds0 + (wm * 64 + r32) * 128 + ((h ^ ((r32 >> 1) & 7)) << 4);
  const int cb = (lane >> 4) & 1, q = (lane >> 2) & 3, p = lane & 3;
  unsigned xb = lds0 + A_B + (8 * h + q) * 128 + ((((2 * cb + (p >> 1)) ^ ((q & 2) << 1))) << 4) + (p & 1) * 8;
  if (NJ == 2) xb += wn * 8192; else xb ^= wn << 6;
  bf16x8 wf[4][2], xf[4][NJ];
  auto rd = [&](int set, unsigned soff) {
    const unsigned aa = (abase ^ (set << 5)) + soff;
#pragma unroll
    for (int i = 0; i < 2; ++i) wf[set][i] = *(const lds_bf16x8*)(size_t)(aa + i * 4096);
#pragma unroll
    for (int j = 0; j < NJ; ++j) {
      const unsigned xa = (xb ^ (j << 6)) + soff + set * 2048;
      s16x4 lo = __builtin_amdgcn_ds_read_tr16_b64_v4i16((lds_s16x4*)(size_t)(xa));
      s16x4 hi = __builtin_amdgcn_ds_read_tr16_b64_v4i16((lds_s16x4*)(size_t)(xa + 512));
      xf[set][j] = __builtin_shufflevector(lo, hi, 0, 1, 2, 3, 4, 5, 6, 7);
    }
  };
  auto mm = [&](int set) {
#pragma unroll
    for (int j = 0; j < NJ; ++j)
#pragma unroll
      for (int i = 0; i < 2; ++i) acc[i][j] = __builtin_amdgcn_mfma_f32_32x32x16_bf16(xf[set][j], wf[set][i], acc[i][j], 0, 0, 0);
  };
  auto il = [&]() {
#pragma unroll
    for (int j = 0; j < 2 * NJ; ++j) {
      __builtin_amdgcn_sched_group_barrier(0x8, 1, 0);
      __builtin_amdgcn_sched_group_barrier(0x2, 2, 0);
      __builtin_amdgcn_sched_group_barrier(0x100, 2, 0);
    }
  };
  __builtin_amdgcn_s_waitcnt(0xC07F);
  BAR();
  PR_MARK(1)
  SCHED0();
  unsigned so = 0;
  unsigned pft[4] = {0, 0, 0, 0};
  rd(0, so); SCHED0(); rd(1, so);
#pragma unroll 1
  for (int kt = 0; kt < NK - 1; ++kt) {
    unsigned sn = so + STAGE_B; if (sn == NST * STAGE_B) sn = 0;
    SCHED0();
    rd(2, so); mm(0); il(); SCHED0();
    rd(3, so); mm(1); il(); SCHED0();
    BAR(); SCHED0();
    if (kt == PR_KT) { PR_MARK(7) }
    if (NJ == 1 && kt == P_WDPF) {
      asm volatile("buffer_load_dword %0, %4, %5, 0 offen\n\tbuffer_load_dword %1, %4, %5, %6 offen\n\tbuffer_load_dword %2, %4, %5, %7 offen\n\tbuffer_load_dword %3, %4, %5, %8 offen"
                   : "=&v"(pft[0]), "=&v"(pft[1]), "=&v"(pft[2]), "=&v"(pft[3]) : "v"(pfo), "s"(rsPf), "s"(7 * D * 2), "s"(14 * D * 2), "s"(21 * D * 2) : "memory");
    }
    SCHED0();
    rd(0, sn); mm(2); il(); SCHED0();
    rd(1, sn); mm(3); il(); SCHED0();
    so = sn;
  }
  rd(2, so); mm(0); il(); SCHED0();
  rd(3, so); mm(1); il(); SCHED0();
  BAR(); SCHED0();
  auto fin = [&](int i, int j) {
#pragma unroll
    for (int set = 2; set < 4; ++set) acc[i][j] = __builtin_amdgcn_mfma_f32_32x32x16_bf16(xf[set][j], wf[set][i], acc[i][j], 0, 0, 0);
  };
  auto st = [&](int i, int j) {
    const unsigned off = poff + (i * 2 * 14 + kb0 + j) * 1024;
#pragma unroll
    for (int u = 0; u < 4; u += 2) {
      unsigned a0 = pack2(acc[i][j][4 * u], acc[i][j][4 * u + 1]), a1 = pack2(acc[i][j][4 * u + 2], acc[i][j][4 * u + 3]);
      unsigned b0 = pack2(acc[i][j][4 * u + 4], acc[i][j][4 * u + 5]), b1 = pack2(acc[i][j][4 * u + 6], acc[i][j][4 * u + 7]);
      auto s0 = __builtin_amdgcn_permlane32_swap(a0, b0, false, false);
      auto s1 = __builtin_amdgcn_permlane32_swap(a1, b1, false, false);
      u32x4 v = {s0[0], s1[0], s0[1], s1[1]};
      __builtin_amdgcn_raw_buffer_store_b128(v, rsP, off + u * 256, 0, 16);
    }
  };
  rdD(de[0], dbase, wn, 0, unit_slot(0), unit_slot(6));
  fin(0, 0); fin(1, 0);
  SCHED0();
  rdD(de[1], dbase, wn, 1, unit_slot(0), unit_slot(6));
  if (NJ == 2) { fin(0, NJ - 1); st(0, 0); SCHED0(); fin(1, NJ - 1); st(1, 0); SCHED0(); st(0, NJ - 1); st(1, NJ - 1); }
  else { st(0, 0); st(1, 0); }
  if (NJ == 1) asm volatile("" ::"v"(pft[0]), "v"(pft[1]), "v"(pft[2]), "v"(pft[3]));
}

__device__ __forceinline__ bf16x8 add_bf16x8(u32x4 a, u32x4 b) {
  u32x4 r;
#pragma unroll
  for (int i = 0; i < 4; ++i) {
    float lo = __builtin_bit_cast(float, a[i] << 16) + __builtin_bit_cast(float, b[i] << 16);
    float hi = __builtin_bit_cast(float, a[i] & 0xffff0000u) + __builtin_bit_cast(float, b[i] & 0xffff0000u);
    r[i] = pack2(lo, hi);
  }
  return __builtin_bit_cast(bf16x8, r);
}

__global__ __launch_bounds__(512, 2) void fnet(const bf16* __restrict__ Wr, const bf16* __restrict__ Wi,
                                               const bf16* __restrict__ x, const bf16* __restrict__ Dr,
                                               const bf16* __restrict__ Di, bf16* __restrict__ y,
                                               char* __restrict__ ws TS_ARG) {
  extern __shared__ __attribute__((aligned(16))) char smem[];
  unsigned long long pc0;
  asm volatile("s_getpc_b64 %0" : "=s"(pc0));
  TS_BEGIN(0)
  asm volatile("" ::"s"(Wr), "s"(Wi), "s"(x), "s"(Dr), "s"(Di), "s"(y), "s"(ws));
  const int b = blockIdx.x;
  const int xcd = b & 7, idx = b >> 3;
  const int mem = idx & 7, nt = mem & 3, ks = mem >> 2, tbl = idx >> 3, tb = xcd * 4 + tbl;
  const int tid = threadIdx.x, lane = tid & 63, wid = __builtin_amdgcn_readfirstlane(tid >> 6);
  const unsigned lds0 = (unsigned)(size_t)smem;
  const __amdgpu_buffer_rsrc_t rsP = __builtin_amdgcn_make_buffer_rsrc((void*)ws, 0, PART_BYTES, 0x00020000);
  gu64* const flags = (gu64*)(ws + SYNC_OFF) + tb * 32;
  const int et = nt, th = ks;
  const __amdgpu_buffer_rsrc_t rsDr = __builtin_amdgcn_make_buffer_rsrc((void*)Dr, 0, D * D * 2, 0x00020000);
  const __amdgpu_buffer_rsrc_t rsDi = __builtin_amdgcn_make_buffer_rsrc((void*)Di, 0, D * D * 2, 0x00020000);
#define STAMP()
#define BART() do { BAR(); STAMP(); } while (0)
  unsigned long long ep;
  auto load_ep = [&]() { ep = *(const volatile __attribute__((address_space(4))) unsigned long long*)(ws + SYNC_OFF + tb * 256 + 64) + 1; };
  const int kg = wid & 3, tt = wid >> 2, im = kg >> 1, hi = kg & 1;
  const int fr = lane & 15, g = lane >> 4;
  const unsigned dbase = lds0 + fr * 128 + ((g ^ (fr >> 1) ^ (hi << 2)) << 4) + im * 16384;
  bf16x8 de[4][7];

  if (__builtin_expect(wid < 4, 1)) {
    const int lw = wid & 3, r5 = lw * 8 + (lane >> 3);
    const __amdgpu_buffer_rsrc_t rsR = __builtin_amdgcn_make_buffer_rsrc((void*)(Wr + (long)tb * 64 * S), 0, 64 * S * 2, 0x00020000);
    const __amdgpu_buffer_rsrc_t rsI = __builtin_amdgcn_make_buffer_rsrc((void*)(Wi + (long)tb * 64 * S), 0, 64 * S * 2, 0x00020000);
    const __amdgpu_buffer_rsrc_t rsX = __builtin_amdgcn_make_buffer_rsrc((void*)x, 0, S * D * 2, 0x00020000);
    const unsigned srcA = (r5 * S + ks * KLEN + (((lane & 7) ^ ((r5 >> 1) & 7)) << 3)) * 2;
    const unsigned srcB = ((ks * KLEN + r5) * D + nt * 128 + (((lane & 7) ^ ((r5 & 2) << 1)) << 3)) * 2;
    const unsigned srcB1 = nt < 3 ? srcB + 128 : 0x7fffff00u;
    const unsigned srcD = ((et * 112 + r5) * D + (((lane & 7) ^ ((r5 >> 1) & 7)) << 3)) * 2;
    const unsigned srcD3 = lw < 2 ? srcD + 96 * D * 2 : 0x7fffff00u;
    auto kof = [&](int p) -> int {
      if (P_ROT == 0) return p;
      if (P_ROT == 1) return (p + 4 * nt) & 15;
      if (nt == 3) return (p + 3 * P_F) & 15;
      int k = p + P_F * nt;
      return p < 3 * P_F ? (k >= 3 * P_F ? k - 3 * P_F : k) : p;
    };
    auto loadW = [&](auto AW, int p, unsigned slot) {
      const unsigned so = kof(p) * 128, dst = lds0 + slot + lw * 1024;
#pragma unroll
      for (int i = 0; i < 2; ++i) dma16<AW()>(rsR, srcA, so + i * (32 * S * 2), dst + i * 4096);
#pragma unroll
      for (int i = 0; i < 2; ++i) dma16<AW()>(rsI, srcA, so + i * (32 * S * 2), dst + 8192 + i * 4096);
    };
    auto loadX = [&](int p, unsigned slot) {
      const unsigned so = kof(p) * (64 * D * 2), dst = lds0 + slot + A_B + lw * 1024;
#pragma unroll
      for (int i = 0; i < 2; ++i) dma16<0>(rsX, srcB, so + i * (32 * D * 2), dst + i * 4096);
#pragma unroll
      for (int i = 0; i < 2; ++i) dma16<0>(rsX, srcB1, so + i * (32 * D * 2), dst + 8192 + i * 4096);
    };
    auto unit = [&](int u, int slot) {
      const unsigned so = u * 128, dst = lds0 + slot * STAGE_B + lw * 1024, dst3 = dst + (lw == 3 ? 3 * 4096 - 1024 : 3 * 4096);
#pragma unroll
      for (int i = 0; i < 3; ++i) dma16<0>(rsDr, srcD, so + i * (32 * D * 2), dst + i * 4096);
      dma16<0>(rsDr, srcD3, so, dst3);
#pragma unroll
      for (int i = 0; i < 3; ++i) dma16<0>(rsDi, srcD, so + i * (32 * D * 2), dst + 16384 + i * 4096);
      dma16<0>(rsDi, srcD3, so, dst3 + 16384);
    };
    std::integral_constant<int, 0> Z; std::integral_constant<int, P_AUXW> AWo;
    STAMP();
    loadW(AWo, 0, 0); loadX(0, 0); loadW(AWo, 1, STAGE_B); loadX(1, STAGE_B);
    WAIT_V(8); BART();
    loadW(AWo, 2, 2 * STAGE_B); loadX(2, 2 * STAGE_B);
    WAIT_V(8); BART();
    loadW(AWo, 3, 3 * STAGE_B); loadX(3, 3 * STAGE_B);
    WAIT_V(8); BART();
    loadW(Z, 4, 4 * STAGE_B); loadX(4, 4 * STAGE_B);
    unsigned slot = 0;
#pragma unroll 1
    for (int p = 5; p < NK; ++p) {
      loadW(Z, p, slot); loadX(p, slot);
      slot += STAGE_B; if (slot == NST * STAGE_B) slot = 0;
      WAIT_V(16); BART();
    }
    load_ep();
#pragma unroll 1
    for (int q = 0; q < 4; ++q) {
      const int u = (0x5160 >> (4 * q)) & 7;
      unit(u, q + 1);
      if (q < 2) { WAIT_V(16); BART(); }
      else if (q == 2) { if (tid == 0) *(lds_u32*)(size_t)(lds0 + CNT_OFF) = 0; WAIT_V(8); WAIT_L(0); BART(); }
    }
    rdD(de[0], dbase, hi, 0, unit_slot(0), unit_slot(6));
    rdD(de[1], dbase, hi, 1, unit_slot(0), unit_slot(6));
    WAIT_L(0); SCHED0(); BART();
#pragma unroll 1
    for (int q = 0; q < 3; ++q) unit((0x423 >> (4 * q)) & 7, q);
    WAIT_V(24);
  } else {
    unsigned xpf = 0;
    const int cw = wid & 3;
    load_ep();
    if (P_XPF && cw == 0) {
      const __amdgpu_buffer_rsrc_t rsX = __builtin_amdgcn_make_buffer_rsrc((void*)x, 0, S * D * 2, 0x00020000);
      asm volatile("buffer_load_dword %0, %1, %2, 0 offen" : "=v"(xpf) : "v"(lane < 56 ? b * (8 * D * 2) + lane * 128 : 0x7ffffff0u), "s"(rsX) : "memory");
    }
    if (cw == 1) {
      const char* pc = (const char*)(pc0 & ~127ull);
      const __amdgpu_buffer_rsrc_t rsC = __builtin_amdgcn_make_buffer_rsrc((void*)pc, 0, CODE_PF_BYTES, 0x00020000);
      asm volatile("buffer_load_dword %0, %1, %2, 0 offen" : "=v"(xpf) : "v"(lane < 4 ? (lane * 32 + idx) * 128 : 0x7ffffff0u), "s"(rsC) : "memory");
    }
    const int wm = cw >> 1, wn = cw & 1, h = lane >> 5, r32 = lane & 31;
    const unsigned poff = ((ks * 256 + wm * 128 + tb * 4 + (r32 >> 4)) * 14) * 1024 + (h * 16 + (r32 & 15)) * 16;
    const unsigned pfo = lane < 49 ? (((tbl * 2 + ks) * 2 + wn) * 28) * (D * 2) + lane * 128 : 0x7ffffff0u;
    if (nt < 3) p1_compute<2>(lds0, cw, lane, nt * 4 + wn * 2, poff, rsP, rsDr, pfo, de, dbase PR_PASS);
    else p1_compute<1>(lds0, cw, lane, 12 + wn, poff, rsP, wm ? rsDi : rsDr, pfo, de, dbase PR_PASS);
    TS_MARK(2, threadIdx.x == 256)
    WAIT_L(0);
    SCHED0();
    BAR();
    WAIT_V(0);
    asm volatile("" ::"v"(xpf));
    if (lane == 0) {
      unsigned old = __hip_atomic_fetch_add((lds_u32*)(size_t)(lds0 + CNT_OFF), 1u, __ATOMIC_RELAXED, __HIP_MEMORY_SCOPE_WORKGROUP);
      if (old == 3) __hip_atomic_store(flags + mem, ep, RLX_AGENT);
    }
    TS_MARK(3, threadIdx.x == 256)
  }
  BART();
  SCHED0();
  rdD(de[2], dbase, hi, 2, unit_slot(1), unit_slot(5));
  rdD(de[3], dbase, hi, 3, unit_slot(1), unit_slot(5));
  if (wid < 4) {
    if (wid == 0) {
      const unsigned long long want = ep;
#pragma unroll 1
      for (unsigned spins = 0; spins < (1u << 22); ++spins) {
        unsigned long long f = __hip_atomic_load(flags + (lane & 7), RLX_AGENT);
        if (__all(f == want)) break;
        __builtin_amdgcn_s_sleep(1);
      }
    }
    WAIT_V(0);
  }
  WAIT_L(0);
  SCHED0();
  BART();
  asm volatile("" ::: "memory");
  u32x4 pf[7][2];
  const unsigned pbase = ((im * 128 + tb * 4 + th * 2 + tt) * 14 + (hi ? 13 : 0)) * 1024 + lane * 16;
  const int pstep = hi ? -1024 : 1024;
  auto ldP = [&](int n) {
#pragma unroll
    for (int s = 0; s < 2; ++s)
      pf[n][s] = __builtin_amdgcn_raw_buffer_load_b128(rsP, pbase + n * pstep, s * (256 * 14 * 1024), 16);
  };
#pragma unroll
  for (int n = 0; n < P_PB0; ++n) ldP(n);
  SCHED0();
  TS_MARK(4, threadIdx.x == 0)
  if (tid == 0 && mem == 0) __hip_atomic_store(flags + 8, ep, RLX_AGENT);
  f32x4 ya[7];
#pragma unroll
  for (int c = 0; c < 7; ++c) ya[c] = f32x4{0.f, 0.f, 0.f, 0.f};
  bf16x8 ps = add_bf16x8(pf[0][0], pf[0][1]);
  SCHED0();
#pragma unroll
  for (int n = 0; n < 7; ++n) {
#pragma unroll
    for (int m = 0; m < P_PBS; ++m) if (P_PB0 + n * P_PBS + m < 7) ldP(P_PB0 + n * P_PBS + m);
    SCHED0();
    bf16x8 pn = ps;
    if (n < 6) pn = add_bf16x8(pf[n + 1][0], pf[n + 1][1]);
#pragma unroll
    for (int c = 0; c < 7; ++c) ya[c] = __builtin_amdgcn_mfma_f32_16x16x32_bf16(de[n & 3][c], ps, ya[c], 0, 0, 0);
#pragma unroll
    for (int c = 0; c < 7; ++c) {
      __builtin_amdgcn_sched_group_barrier(0x8, 1, 0);
      __builtin_amdgcn_sched_group_barrier(0x2, 4, 0);
    }
    SCHED0();
    if (n == 0) rdD(de[0], dbase, hi, 4, unit_slot(2), unit_slot(4));
    if (n == 1) rdD(de[1], dbase, hi, 5, unit_slot(2), unit_slot(4));
    if (n == 2) rdD(de[2], dbase, hi, 6, unit_slot(3), unit_slot(3));
    SCHED0();
    ps = pn;
  }
  TS_MARK(5, threadIdx.x == 0)
  STAMP();
  {
    const unsigned wb = lds0 + RED_OFF + (kg * 32 + tt * 16 + fr) * RED_LD + g * 16;
#pragma unroll
    for (int c = 0; c < 7; ++c) *(lds_f32x4*)(size_t)(wb + c * 64) = ya[c];
  }
  WAIT_L(0);
  BART();
  if (tid < 448) {
    const int t = tid / 14, ch = tid - t * 14;
    const unsigned rb = lds0 + RED_OFF + t * RED_LD + ch * 32;
    f32x4 o[2];
#pragma unroll
    for (int z = 0; z < 2; ++z) {
      f32x4 r0 = *(const lds_f32x4*)(size_t)(rb + z * 16), r1 = *(const lds_f32x4*)(size_t)(rb + z * 16 + 32 * RED_LD);
      f32x4 r2 = *(const lds_f32x4*)(size_t)(rb + z * 16 + 64 * RED_LD), r3 = *(const lds_f32x4*)(size_t)(rb + z * 16 + 96 * RED_LD);
      o[z] = (r0 + r1) - (r2 + r3);
    }
    const __amdgpu_buffer_rsrc_t rsY = __builtin_amdgcn_make_buffer_rsrc((void*)y, 0, S * D * 2, 0x00020000);
    u32x4 v = {pack2(o[0][0], o[0][1]), pack2(o[0][2], o[0][3]), pack2(o[1][0], o[1][1]), pack2(o[1][2], o[1][3])};
    __builtin_amdgcn_raw_buffer_store_b128(v, rsY, ((tb * 64 + th * 32 + t) * D + et * 112 + ch * 8) * 2, 0, P_YAUX);
  }
  STAMP();
  TS_MARK(6, threadIdx.x == 0)
  TS_END(0)
}

extern "C" void kernel_launch(void* const* d_in, const int* in_sizes, int n_in, void* d_out, int out_size,
                              void* d_ws, size_t ws_size, hipStream_t stream) {
  const bf16* x = (const bf16*)d_in[0];
  const bf16* Wr = (const bf16*)d_in[1];
  const bf16* Wi = (const bf16*)d_in[2];
  const bf16* Dr = (const bf16*)d_in[3];
  const bf16* Di = (const bf16*)d_in[4];
  (void)hipFuncSetAttribute((const void*)fnet, hipFuncAttributeMaxDynamicSharedMemorySize, LDS_BYTES);
  fnet<<<256, 512, LDS_BYTES, stream>>>(Wr, Wi, x, Dr, Di, (bf16*)d_out, (char*)d_ws TS_PASS);
  PROBE_TAIL
}
```

```cpp
#include <hip/hip_runtime.h>
#include <hip/hip_bf16.h>
#include <stdint.h>
typedef __hip_bfloat16 bf16;
typedef __attribute__((ext_vector_type(8))) short bf16x8;
typedef __attribute__((ext_vector_type(4))) short s16x4;
typedef __attribute__((ext_vector_type(4))) float f32x4;
typedef __attribute__((ext_vector_type(16))) float f32x16;
typedef __attribute__((ext_vector_type(4))) unsigned u32x4;
typedef __attribute__((ext_vector_type(2))) float f32x2;
typedef __attribute__((ext_vector_type(2))) __bf16 bf16x2_t;
typedef __attribute__((address_space(3))) s16x4 lds_s16x4;
typedef __attribute__((address_space(3))) bf16x8 lds_bf16x8;
typedef __attribute__((address_space(3))) f32x4 lds_f32x4;
typedef __attribute__((address_space(3))) unsigned lds_u32;
typedef __attribute__((address_space(3))) u32x4 lds_u32x4;
typedef __attribute__((address_space(1))) unsigned long long gu64;
typedef __amdgpu_buffer_rsrc_t rsrc_t;
#define RLX_AGENT __ATOMIC_RELAXED, __HIP_MEMORY_SCOPE_AGENT

constexpr int S = 2048, D = 448;
#define WAIT_V(n) asm volatile("s_waitcnt vmcnt(%0)" ::"n"(n) : "memory")
#define WAIT_L(n) asm volatile("s_waitcnt lgkmcnt(%0)" ::"n"(n) : "memory")
#define SCHED0() __builtin_amdgcn_sched_barrier(0)
#define BAR() __builtin_amdgcn_s_barrier()
#define MKRS(p, bytes) __builtin_amdgcn_make_buffer_rsrc((void*)(p), 0, (bytes), 0x00020000)

#define TS_BEGIN(k)
#define TS_END(k)
#define TS_MARK(k, cond)
#define TS_ARG
#define TS_PASS
#define P_WDPF 3
#define P_PB0 3
#define P_PD 4
#define P_LM 2
#define CODE_PF_BYTES 11520

__device__ __forceinline__ unsigned pack2(float a, float b) {
  bf16x2_t v = __builtin_convertvector(f32x2{a, b}, bf16x2_t);
  return __builtin_bit_cast(unsigned, v);
}

__device__ __forceinline__ void dma16(rsrc_t rs, unsigned voff, unsigned soff, unsigned lds) {
  asm volatile("s_mov_b32 m0, %0\n\ts_nop 0\n\tbuffer_load_dwordx4 %1, %2, %3 offen lds" ::"s"(lds), "v"(voff), "s"(rs), "s"(soff) : "memory");
}

constexpr int KS = 2, KLEN = S / KS, NK = KLEN / 64;
constexpr int A_B = 128 * 128, STAGE_B = 32768;
constexpr int LDS_BYTES = 160 * 1024;
constexpr int PART_BYTES = KS * 256 * 14 * 1024;
constexpr int SYNC_OFF = 16 << 20;
constexpr int NST = 5;
constexpr int UN = 7;
__host__ __device__ constexpr int unit_slot(int u) { return (u + 1) % 5; }
constexpr int RED_OFF = 3 * STAGE_B;
constexpr int SIG_OFF = STAGE_B + 15360, CNT_OFF = SIG_OFF + 32;

__device__ __forceinline__ void sig(unsigned addr, unsigned val) {
  asm volatile("s_mov_b64 exec, 1\n\tds_write_b32 %0, %1\n\ts_mov_b64 exec, -1" ::"v"(addr), "v"(val) : "memory");
}
__device__ __forceinline__ void wait_sig(unsigned addr, unsigned target) {
#pragma unroll 1
  for (unsigned spins = 0; spins < (1u << 24); ++spins) {
    const u32x4 v = *(const volatile lds_u32x4*)(size_t)addr;
    const unsigned m = min(min(v[0], v[1]), min(v[2], v[3]));
    if (__builtin_amdgcn_readfirstlane(m) >= target) break;
    __builtin_amdgcn_s_sleep(1);
  }
  asm volatile("" ::: "memory");
}

__device__ __forceinline__ void rdD(bf16x8* d, unsigned dbase, int n) {
  const unsigned eb = dbase + unit_slot(n) * STAGE_B;
#pragma unroll
  for (int c = 0; c < 7; ++c) d[c] = *(const lds_bf16x8*)(size_t)(eb + c * 2048);
}

template <int NJ>
__device__ __forceinline__ void p1_compute(unsigned lds0, int wid, int lane, int kb0, unsigned poff, rsrc_t rsP, rsrc_t rsPf, unsigned pfo) {
  f32x16 acc[2][NJ];
#pragma unroll
  for (int i = 0; i < 2; ++i)
#pragma unroll
    for (int j = 0; j < NJ; ++j)
#pragma unroll
      for (int r = 0; r < 16; ++r) acc[i][j][r] = 0.f;
  const int wm = wid >> 1, wn = wid & 1;
  const int h = lane >> 5, r32 = lane & 31;
  const unsigned abase = lds0 + (wm * 64 + r32) * 128 + ((h ^ ((r32 >> 1) & 7)) << 4);
  const int cb = (lane >> 4) & 1, q = (lane >> 2) & 3, p = lane & 3;
  unsigned xb = lds0 + A_B + (8 * h + q) * 128 + ((((2 * cb + (p >> 1)) ^ ((q & 2) << 1))) << 4) + (p & 1) * 8;
  if (NJ == 2) xb += wn * 8192; else xb ^= wn << 6;
  bf16x8 wf[4][2], xf[4][NJ];
  auto rdx = [&](int set, int j, unsigned soff) {
    const unsigned xa = (xb ^ (j << 6)) + soff + set * 2048;
    s16x4 lo = __builtin_amdgcn_ds_read_tr16_b64_v4i16((lds_s16x4*)(size_t)(xa));
    s16x4 hi = __builtin_amdgcn_ds_read_tr16_b64_v4i16((lds_s16x4*)(size_t)(xa + 512));
    return __builtin_shufflevector(lo, hi, 0, 1, 2, 3, 4, 5, 6, 7);
  };
  auto rd = [&](int set, unsigned soff) {
    const unsigned aa = (abase ^ (set << 5)) + soff;
#pragma unroll
    for (int i = 0; i < 2; ++i) wf[set][i] = *(const lds_bf16x8*)(size_t)(aa + i * 4096);
#pragma unroll
    for (int j = 0; j < NJ; ++j) xf[set][j] = rdx(set, j, soff);
  };
  auto mm = [&](int set) {
#pragma unroll
    for (int j = 0; j < NJ; ++j)
#pragma unroll
      for (int i = 0; i < 2; ++i) acc[i][j] = __builtin_amdgcn_mfma_f32_32x32x16_bf16(xf[set][j], wf[set][i], acc[i][j], 0, 0, 0);
  };
  auto il = [&]() {
#pragma unroll
    for (int j = 0; j < 2 * NJ; ++j) {
      __builtin_amdgcn_sched_group_barrier(0x8, 1, 0);
      __builtin_amdgcn_sched_group_barrier(0x2, 2, 0);
      __builtin_amdgcn_sched_group_barrier(0x100, 2, 0);
    }
  };
  __builtin_amdgcn_s_waitcnt(0xC07F);
  BAR();
  SCHED0();
  unsigned so = 0;
  unsigned pft[4] = {0, 0, 0, 0};
  rd(0, so); SCHED0(); rd(1, so);
#pragma unroll 1
  for (int kt = 0; kt < NK - 3; ++kt) {
    unsigned sn = so + STAGE_B; if (sn == NST * STAGE_B) sn = 0;
    SCHED0();
    rd(2, so); mm(0); il(); SCHED0();
    rd(3, so); mm(1); il(); SCHED0();
    BAR(); SCHED0();
    if (NJ == 1 && kt == P_WDPF) {
      asm volatile("buffer_load_dword %0, %4, %5, 0 offen\n\tbuffer_load_dword %1, %4, %5, %6 offen\n\tbuffer_load_dword %2, %4, %5, %7 offen\n\tbuffer_load_dword %3, %4, %5, %8 offen"
                   : "=&v"(pft[0]), "=&v"(pft[1]), "=&v"(pft[2]), "=&v"(pft[3]) : "v"(pfo), "s"(rsPf), "s"(7 * D * 2), "s"(14 * D * 2), "s"(21 * D * 2) : "memory");
    }
    SCHED0();
    rd(0, sn); mm(2); il(); SCHED0();
    rd(1, sn); mm(3); il(); SCHED0();
    so = sn;
  }
  rd(2, so); mm(0); il(); SCHED0();
  rd(3, so); mm(1); il(); SCHED0();
  if (NJ == 1) { WAIT_V(0); asm volatile("" ::"v"(pft[0]), "v"(pft[1]), "v"(pft[2]), "v"(pft[3])); }
  BAR(); SCHED0();
  const unsigned sga = lds0 + SIG_OFF + 16 + wid * 4;
  sig(sga, 1);
  constexpr int NH = 8 * NJ;
  bf16x8 w2[8][2], x2[NH];
  auto rdh = [&](int hh) {
    const unsigned soff = (hh & 4) ? ((NK - 1) % NST) * STAGE_B : ((NK - 2) % NST) * STAGE_B;
    if (hh < 8) {
      const unsigned aa = (abase ^ ((hh & 3) << 5)) + soff;
#pragma unroll
      for (int i = 0; i < 2; ++i) w2[hh & 7][i] = *(const lds_bf16x8*)(size_t)(aa + i * 4096);
    }
    x2[hh] = rdx(hh & 3, hh >> 3, soff);
  };
  auto mh = [&](int hh, int i) { acc[i][hh >> 3] = __builtin_amdgcn_mfma_f32_32x32x16_bf16(x2[hh], w2[hh & 7][i], acc[i][hh >> 3], 0, 0, 0); };
  auto st = [&](int i, int j) {
    const unsigned off = poff + (i * 2 * 14 + kb0 + j) * 1024;
#pragma unroll
    for (int u = 0; u < 4; u += 2) {
      unsigned a0 = pack2(acc[i][j][4 * u], acc[i][j][4 * u + 1]), a1 = pack2(acc[i][j][4 * u + 2], acc[i][j][4 * u + 3]);
      unsigned b0 = pack2(acc[i][j][4 * u + 4], acc[i][j][4 * u + 5]), b1 = pack2(acc[i][j][4 * u + 6], acc[i][j][4 * u + 7]);
      auto s0 = __builtin_amdgcn_permlane32_swap(a0, b0, false, false);
      auto s1 = __builtin_amdgcn_permlane32_swap(a1, b1, false, false);
      u32x4 v = {s0[0], s1[0], s0[1], s1[1]};
      __builtin_amdgcn_raw_buffer_store_b128(v, rsP, off + u * 256, 0, 16);
    }
  };
#pragma unroll
  for (int hh = 0; hh < P_PD / 2; ++hh) rdh(hh);
  mm(2); il(); SCHED0();
#pragma unroll
  for (int hh = P_PD / 2; hh < P_PD; ++hh) rdh(hh);
  mm(3); il(); SCHED0();
  if (P_PD > NH - 5) sig(sga, 2);
#pragma unroll
  for (int hh = 0; hh < NH - P_LM; ++hh) {
    if (hh + P_PD < NH) rdh(hh + P_PD);
    mh(hh, 0); mh(hh, 1);
    if (NJ == 2 && hh == 9) st(0, 0);
    if (NJ == 2 && hh == 11) st(1, 0);
#pragma unroll
    for (int j = 0; j < 2; ++j) {
      __builtin_amdgcn_sched_group_barrier(0x8, 1, 0);
      __builtin_amdgcn_sched_group_barrier(0x100, 2, 0);
      __builtin_amdgcn_sched_group_barrier(0x2, 6, 0);
      __builtin_amdgcn_sched_group_barrier(0x40, 1, 0);
    }
    SCHED0();
    if (hh + P_PD == NH - 5) sig(sga, 2);
    SCHED0();
  }
#pragma unroll
  for (int hh = NH - P_LM; hh < NH; ++hh) mh(hh, 0);
  SCHED0();
#pragma unroll
  for (int hh = NH - P_LM; hh < NH; ++hh) mh(hh, 1);
  st(0, NJ - 1);
  SCHED0();
  st(1, NJ - 1);
  SCHED0();
  sig(sga, 3);
}

__device__ __forceinline__ bf16x8 add_bf16x8(u32x4 a, u32x4 b, float one) {
  u32x4 r;
#pragma unroll
  for (int i = 0; i < 4; ++i) {
    float lo = __builtin_fmaf(__builtin_bit_cast(float, a[i] << 16), one, __builtin_bit_cast(float, b[i] << 16));
    float hi = __builtin_fmaf(__builtin_bit_cast(float, a[i] & 0xffff0000u), one, __builtin_bit_cast(float, b[i] & 0xffff0000u));
    asm("" : "+v"(lo));
    r[i] = pack2(lo, hi);
  }
  return __builtin_bit_cast(bf16x8, r);
}

__global__ __launch_bounds__(512, 2) void fnet(const bf16* Wr_, const bf16* Wi_, const bf16* x_, const bf16* Dr_, const bf16* Di_, bf16* y_, char* ws_ TS_ARG) {
  extern __shared__ __attribute__((aligned(16))) char smem[];
  TS_BEGIN(0)
  typedef const volatile __attribute__((address_space(4))) unsigned long long karg_t;
  karg_t* const kp = (karg_t*)__builtin_amdgcn_kernarg_segment_ptr();
  const char* const Wr = (const char*)kp[0];
  const char* const Wi = (const char*)kp[1];
  const char* const x = (const char*)kp[2];
  const char* const Dr = (const char*)kp[3];
  const char* const Di = (const char*)kp[4];
  char* const y = (char*)kp[5];
  char* const ws = (char*)kp[6];
  unsigned long long pc0;
  asm volatile("s_getpc_b64 %0" : "=s"(pc0));
  int b = blockIdx.x, tid = threadIdx.x;
  asm volatile("" : "+s"(b), "+v"(tid));
  SCHED0();
  const int xcd = b & 7, idx = b >> 3;
  const int mem = idx & 7, nt = mem & 3, ks = mem >> 2, tbl = idx >> 3, tb = xcd * 4 + tbl;
  const int lane = tid & 63, wid = __builtin_amdgcn_readfirstlane(tid >> 6);
  const unsigned lds0 = (unsigned)(size_t)smem;
  const int lw = wid & 3, r5 = lw * 8 + (lane >> 3);
  unsigned srcA = ((tb * 64 + r5) * S + ks * KLEN + (((lane & 7) ^ ((r5 >> 1) & 7)) << 3)) * 2;
  unsigned srcB = ((ks * KLEN + r5) * D + nt * 128 + (((lane & 7) ^ ((r5 & 2) << 1)) << 3)) * 2;
  unsigned ldw = lds0 + lw * 1024, k0 = 4 * nt;
  asm volatile("" : "+v"(srcA), "+v"(srcB), "+s"(ldw), "+s"(k0));
  SCHED0();
  const int et = nt, th = ks;
#define STAMP()
#define BART() do { BAR(); STAMP(); } while (0)
#define STAMP2()
  unsigned long long ep;
  auto load_ep = [&]() { ep = *(const volatile __attribute__((address_space(4))) unsigned long long*)(ws + SYNC_OFF + tb * 256 + 64) + 1; };
  const int par = wid >> 2, im = (wid >> 1) & 1, tt = wid & 1, kg = im * 2 + par;
  unsigned dbase;
  auto mk_dbase = [&]() {
    int l = lane;
    asm volatile("" : "+v"(l));
    dbase = lds0 + (l & 15) * 128 + (((l >> 4) ^ ((l & 15) >> 1) ^ (par << 2)) << 4) + im * 16384;
  };
  bf16x8 de[4][7];
  const unsigned sgL = lds0 + SIG_OFF, sgC = sgL + 16;

  if (__builtin_expect(wid < 4, 1)) {
    const rsrc_t rsR = MKRS(Wr, S * S * 2), rsI = MKRS(Wi, S * S * 2), rsX = MKRS(x, S * D * 2);
#define WAIT_S(ns) do { if (nt == 3) WAIT_V(6 * (ns)); else WAIT_V(8 * (ns)); } while (0)
    auto stage = [&](int p, unsigned slot) {
      const unsigned kk = (p + k0) & 15, sw = kk * 128, sx = kk * (64 * D * 2), dst = ldw + slot;
#pragma unroll
      for (int i = 0; i < 2; ++i) dma16(rsR, srcA, sw + i * (32 * S * 2), dst + i * 4096);
#pragma unroll
      for (int i = 0; i < 2; ++i) dma16(rsI, srcA, sw + i * (32 * S * 2), dst + 8192 + i * 4096);
#pragma unroll
      for (int i = 0; i < 2; ++i) dma16(rsX, srcB, sx + i * (32 * D * 2), dst + A_B + i * 4096);
      if (nt < 3) {
#pragma unroll
        for (int i = 0; i < 2; ++i) dma16(rsX, srcB, sx + 128 + i * (32 * D * 2), dst + A_B + 8192 + i * 4096);
      }
    };
    STAMP();
    unsigned slot = 0;
#pragma unroll 1
    for (int p = 0; p < NK; ++p) {
      stage(p, slot);
      slot += STAGE_B; if (slot == NST * STAGE_B) slot = 0;
      if (p >= 5) { WAIT_S(2); BART(); }
      else if (p >= 1 && p <= 3) { WAIT_S(1); BART(); }
    }
    load_ep();
    mk_dbase();
    const rsrc_t rsDr = MKRS(Dr, D * D * 2), rsDi = MKRS(Di, D * D * 2);
    const unsigned srcD = ((et * 112 + r5) * D + (((lane & 7) ^ ((r5 >> 1) & 7)) << 3)) * 2;
    const unsigned srcD3 = lw < 2 ? srcD + 96 * D * 2 : srcD - 16 * D * 2;
    const rsrc_t rsD3 = lw < 2 ? rsDr : rsDi;
    const unsigned sgw = sgL + wid * 4;
    if (tid < 16) *(lds_u32*)(size_t)(sgL + tid * 4) = 0;
    auto unit = [&](int u, int sl) {
      const unsigned so = u * 128, dst = ldw + sl * STAGE_B;
#pragma unroll
      for (int i = 0; i < 3; ++i) dma16(rsDr, srcD, so + i * (32 * D * 2), dst + i * 4096);
      dma16(rsD3, srcD3, so, dst + (lw < 2 ? 3 * 4096 : 16384 - 2048));
#pragma unroll
      for (int i = 0; i < 3; ++i) dma16(rsDi, srcD, so + (16 + i * 32) * (D * 2), dst + 16384 + 2048 + i * 4096);
    };
#pragma unroll 1
    for (int u = 0; u < 4; ++u) {
      if (u >= 2) wait_sig(sgC, u - 1);
      unit(u, u + 1);
      if (u == 0) { WAIT_V(UN); WAIT_L(0); BAR(); }
      if (u == 2) { WAIT_V(UN); sig(sgw, 1); }
      if (u != 3) STAMP();
    }
    wait_sig(sgL, 1);
    rdD(de[0], dbase, 0);
    rdD(de[1], dbase, 1);
    sig(sgw, 2);
#pragma unroll 1
    for (int u = 4; u < 7; ++u) {
      if (u < 6) wait_sig(sgC, u - 1);
      if (u == 5) wait_sig(sgL, 2);
      unit(u, u - 4);
      if (u != 5) STAMP();
    }
    WAIT_V(3 * UN);
    sig(sgw, 3);
    STAMP2();
  } else {
    unsigned xpf = 0;
    const int cw = wid & 3;
    load_ep();
    if (cw == 0) {
      asm volatile("buffer_load_dword %0, %1, %2, 0 offen" : "=v"(xpf) : "v"(lane < 56 ? b * (8 * D * 2) + lane * 128 : 0x7ffffff0u), "s"(MKRS(x, S * D * 2)) : "memory");
    }
    if (cw == 1) {
      asm volatile("buffer_load_dword %0, %1, %2, 0 offen" : "=v"(xpf) : "v"(lane < 4 ? (lane * 32 + idx) * 128 : 0x7ffffff0u), "s"(MKRS(pc0 & ~127ull, CODE_PF_BYTES)) : "memory");
    }
    mk_dbase();
    const rsrc_t rsP = MKRS(ws, PART_BYTES);
    const int wm = cw >> 1, wn = cw & 1, h = lane >> 5, r32 = lane & 31;
    const unsigned poff = ((ks * 256 + wm * 128 + tb * 4 + (r32 >> 4)) * 14) * 1024 + (h * 16 + (r32 & 15)) * 16;
    const unsigned pfo = lane < 49 ? (((tbl * 2 + ks) * 2 + wn) * 28) * (D * 2) + lane * 128 : 0x7ffffff0u;
    if (nt < 3) p1_compute<2>(lds0, cw, lane, nt * 4 + wn * 2, poff, rsP, rsP, pfo);
    else p1_compute<1>(lds0, cw, lane, 12 + wn, poff, rsP, MKRS(wm ? Di : Dr, D * D * 2), pfo);
    TS_MARK(2, threadIdx.x == 256)
    wait_sig(sgL, 1);
    rdD(de[0], dbase, 0);
    rdD(de[1], dbase, 1);
    sig(sgL + wid * 4, 4);
    WAIT_V(0);
    asm volatile("" ::"v"(xpf));
    if (lane == 0) {
      unsigned old = __hip_atomic_fetch_add((lds_u32*)(size_t)(lds0 + CNT_OFF), 1u, __ATOMIC_RELAXED, __HIP_MEMORY_SCOPE_WORKGROUP);
      if (old == 3) __hip_atomic_store((gu64*)(ws + SYNC_OFF) + tb * 32 + mem, ep, RLX_AGENT);
    }
    TS_MARK(3, threadIdx.x == 256)
    STAMP2();
  }
  gu64* const flags = (gu64*)(ws + SYNC_OFF) + tb * 32;
  wait_sig(sgL, 3);
  rdD(de[2], dbase, 2);
  rdD(de[3], dbase, 3);
  SCHED0();
  STAMP2();
  if (wid < 4) {
    if (wid == 0) {
#pragma unroll 1
      for (unsigned spins = 0; spins < (1u << 22); ++spins) {
        unsigned long long f = __hip_atomic_load(flags + (lane & 7), RLX_AGENT);
        if (__all(f == ep)) break;
        __builtin_amdgcn_s_sleep(1);
      }
    }
    WAIT_V(0);
  }
  const rsrc_t rsP = MKRS(ws, PART_BYTES);
  unsigned pbase = ((im * 128 + tb * 4 + th * 2 + tt) * 14 + par) * 1024 + lane * 16;
  asm volatile("" : "+v"(pbase));
  WAIT_L(0);
  SCHED0();
  STAMP2();
  BART();
  STAMP2();
  asm volatile("" ::: "memory");
  u32x4 pf[7][2];
  auto ldP = [&](int n) {
#pragma unroll
    for (int s = 0; s < 2; ++s)
      pf[n][s] = __builtin_amdgcn_raw_buffer_load_b128(rsP, pbase, n * 2048 + s * (256 * 14 * 1024), 16);
    SCHED0();
  };
#pragma unroll
  for (int n = 0; n < P_PB0; ++n) ldP(n);
  SCHED0();
  TS_MARK(4, threadIdx.x == 0)
  f32x4 ya[7];
#pragma unroll
  for (int c = 0; c < 7; ++c) ya[c] = f32x4{0.f, 0.f, 0.f, 0.f};
  float one = 1.0f;
  asm volatile("" : "+s"(one));
  int tl = tid;
  asm volatile("" : "+v"(tl));
  const int fr = tl & 15, g = (tl >> 4) & 3;
  unsigned wa[4];
#pragma unroll
  for (int c = 0; c < 4; ++c) {
    wa[c] = lds0 + RED_OFF + (kg * 32 + tt * 16 + fr) * 512 + (g & 1) * 256 + (((2 * c + (g >> 1)) ^ (fr & 7)) << 4);
    asm volatile("" : "+v"(wa[c]));
  }
  unsigned rb = lds0 + RED_OFF + (tl >> 4) * 512 + ((fr ^ ((tl >> 4) & 7)) << 4);
  unsigned yo = fr < 14 ? ((tb * 64 + th * 32 + (tl >> 4)) * D + et * 112 + fr * 8) * 2 : 0x7ffffff0u;
  asm volatile("" : "+v"(rb), "+v"(yo));
  SCHED0();
  bf16x8 ps = add_bf16x8(pf[0][0], pf[0][1], one);
  SCHED0();
  STAMP2();
#pragma unroll
  for (int n = 0; n < 7; ++n) {
    if (P_PB0 + n < 7) ldP(P_PB0 + n);
    if (P_PB0 + n == 7 && tid == 0 && mem == 0) __hip_atomic_store(flags + 8, ep, RLX_AGENT);
    SCHED0();
    bf16x8 pn = ps;
    if (n < 6) pn = add_bf16x8(pf[n + 1][0], pf[n + 1][1], one);
#pragma unroll
    for (int c = 0; c < 7; ++c) ya[c] = __builtin_amdgcn_mfma_f32_16x16x32_bf16(de[n & 3][c], ps, ya[c], 0, 0, 0);
    if (n < 6) {
#pragma unroll
      for (int c = 0; c < 7; ++c) {
        __builtin_amdgcn_sched_group_barrier(0x8, 1, 0);
        __builtin_amdgcn_sched_group_barrier(0x2, 4, 0);
      }
    } else {
#pragma unroll
      for (int c = 0; c < 7; ++c) *(lds_f32x4*)(size_t)(wa[c & 3] + (c >> 2) * 128) = ya[c];
      __builtin_amdgcn_sched_group_barrier(0x8, 3, 0);
#pragma unroll
      for (int c = 0; c < 4; ++c) {
        __builtin_amdgcn_sched_group_barrier(0x200, 1, 0);
        __builtin_amdgcn_sched_group_barrier(0x8, 1, 0);
      }
      __builtin_amdgcn_sched_group_barrier(0x200, 3, 0);
    }
    SCHED0();
    if (n < 3) rdD(de[n], dbase, n + 4);
    SCHED0();
    ps = pn;
    STAMP2();
  }
  TS_MARK(5, threadIdx.x == 0)
  STAMP();
  WAIT_L(0);
  STAMP2();
  BART();
  STAMP2();
  {
    f32x4 r[2][4], o[2];
#pragma unroll
    for (int z = 0; z < 2; ++z)
#pragma unroll
      for (int k = 0; k < 4; ++k) r[z][k] = *(const lds_f32x4*)(size_t)(rb + z * 256 + k * (32 * 512));
    SCHED0();
#pragma unroll
    for (int z = 0; z < 2; ++z) o[z] = (r[z][0] + r[z][1]) - (r[z][2] + r[z][3]);
    u32x4 v = {pack2(o[0][0], o[0][1]), pack2(o[0][2], o[0][3]), pack2(o[1][0], o[1][1]), pack2(o[1][2], o[1][3])};
    __builtin_amdgcn_raw_buffer_store_b128(v, MKRS(y, S * D * 2), yo, 0, 0);
  }
  STAMP();
  TS_MARK(6, threadIdx.x == 0)
  TS_END(0)
}

extern "C" void kernel_launch(void* const* d_in, const int* in_sizes, int n_in, void* d_out, int out_size,
                              void* d_ws, size_t ws_size, hipStream_t stream) {
  const bf16* x = (const bf16*)d_in[0];
  const bf16* Wr = (const bf16*)d_in[1];
  const bf16* Wi = (const bf16*)d_in[2];
  const bf16* Dr = (const bf16*)d_in[3];
  const bf16* Di = (const bf16*)d_in[4];
  (void)hipFuncSetAttribute((const void*)fnet, hipFuncAttributeMaxDynamicSharedMemorySize, LDS_BYTES);
  fnet<<<256, 512, LDS_BYTES, stream>>>(Wr, Wi, x, Dr, Di, (bf16*)d_out, (char*)d_ws TS_PASS);
}
```

```cpp
#include <hip/hip_runtime.h>
#include <hip/hip_bf16.h>
#include <stdint.h>
typedef __hip_bfloat16 bf16;
typedef __attribute__((ext_vector_type(8))) short bf16x8;
typedef __attribute__((ext_vector_type(4))) short s16x4;
typedef __attribute__((ext_vector_type(4))) float f32x4;
typedef __attribute__((ext_vector_type(4))) unsigned u32x4;
typedef __attribute__((ext_vector_type(2))) float f32x2;
typedef __attribute__((ext_vector_type(2))) __bf16 bf16x2_t;
typedef __attribute__((address_space(3))) s16x4 lds_s16x4;
typedef __attribute__((address_space(3))) bf16x8 lds_bf16x8;
typedef __attribute__((address_space(3))) f32x4 lds_f32x4;
typedef __attribute__((address_space(3))) unsigned lds_u32;
typedef __attribute__((address_space(3))) u32x4 lds_u32x4;
typedef __attribute__((address_space(1))) unsigned long long gu64;
typedef __amdgpu_buffer_rsrc_t rsrc_t;
#define RLX_AGENT __ATOMIC_RELAXED, __HIP_MEMORY_SCOPE_AGENT

constexpr int S = 2048, D = 448;
#define WAIT_V(n) asm volatile("s_waitcnt vmcnt(%0)" ::"n"(n) : "memory")
#define WAIT_L(n) asm volatile("s_waitcnt lgkmcnt(%0)" ::"n"(n) : "memory")
#define SCHED0() __builtin_amdgcn_sched_barrier(0)
#define BAR() __builtin_amdgcn_s_barrier()
#define MKRS(p, bytes) __builtin_amdgcn_make_buffer_rsrc((void*)(p), 0, (bytes), 0x00020000)

constexpr int WDPF_STEP = 3;
constexpr int PB0 = 3;
constexpr int CODE_PF_BYTES = 11432;

__device__ __forceinline__ unsigned pack2(float a, float b) {
  bf16x2_t v = __builtin_convertvector(f32x2{a, b}, bf16x2_t);
  return __builtin_bit_cast(unsigned, v);
}

__device__ __forceinline__ void dma16(rsrc_t rs, unsigned voff, unsigned soff, unsigned lds) {
  asm volatile("s_mov_b32 m0, %0\n\ts_nop 0\n\tbuffer_load_dwordx4 %1, %2, %3 offen lds" ::"s"(lds), "v"(voff), "s"(rs), "s"(soff) : "memory");
}

constexpr int KS = 2, KLEN = S / KS, NK = KLEN / 64;
constexpr int A_B = 128 * 128, STAGE_B = 32768;
constexpr int LDS_BYTES = 160 * 1024;
constexpr int PBLK = 14 * 1024 + 128;
constexpr int PART_BYTES = KS * 256 * PBLK;
constexpr int SYNC_OFF = 16 << 20;
constexpr int NST = 5;
constexpr int UN = 7;
__host__ __device__ constexpr int unit_slot(int u) { return (u + 1) % 5; }
constexpr int RED_OFF = 3 * STAGE_B;
constexpr int SIG_OFF = STAGE_B + 15360, CNT_OFF = SIG_OFF + 32;

__device__ __forceinline__ void sig(unsigned addr, unsigned val) {
  asm volatile("s_mov_b64 exec, 1\n\tds_write_b32 %0, %1\n\ts_mov_b64 exec, -1" ::"v"(addr), "v"(val) : "memory");
}
__device__ __forceinline__ void wait_sig(unsigned addr, unsigned target) {
#pragma unroll 1
  for (unsigned spins = 0; spins < (1u << 24); ++spins) {
    const u32x4 v = *(const volatile lds_u32x4*)(size_t)addr;
    const unsigned m = min(min(v[0], v[1]), min(v[2], v[3]));
    if (__builtin_amdgcn_readfirstlane(m) >= target) break;
    __builtin_amdgcn_s_sleep(1);
  }
  asm volatile("" ::: "memory");
}

__device__ __forceinline__ void rdD(bf16x8* d, unsigned dbase, int n) {
  const unsigned eb = dbase + unit_slot(n) * STAGE_B;
#pragma unroll
  for (int c = 0; c < 7; ++c) d[c] = *(const lds_bf16x8*)(size_t)(eb + c * 2048);
}

#define SGB(mask, n) __builtin_amdgcn_sched_group_barrier(mask, n, 0)
template <int NJ>
__device__ __forceinline__ void p1_compute(unsigned lds0, int wid, int lane, int kb0, unsigned poff, rsrc_t rsP, rsrc_t rsPf, unsigned pfo) {
  constexpr int NC = 2 * NJ;
  f32x4 acc[4][NC];
#pragma unroll
  for (int i = 0; i < 4; ++i)
#pragma unroll
    for (int c = 0; c < NC; ++c) { acc[i][c] = f32x4{0.f, 0.f, 0.f, 0.f}; asm volatile("" : "+v"(acc[i][c])); }
  const int wm = wid >> 1, wn = wid & 1;
  const int fr = lane & 15, g = lane >> 4, q = (lane >> 2) & 3, p = lane & 3;
  const unsigned ab = lds0 + (wm * 64 + fr) * 128 + ((g ^ (fr >> 1)) << 4);
  unsigned xb = lds0 + A_B + (8 * g + q) * 128 + (p & 1) * 8 + (((p >> 1) ^ (q & 2) ^ ((g & 1) << 2)) << 4);
  if (NJ == 2) xb += wn * 8192; else xb ^= wn << 6;
  unsigned ba[2], bx[NC], ca[2], cx[NC];
#pragma unroll
  for (int t = 0; t < 2; ++t) { ba[t] = ab ^ (t << 6); asm("" : "+v"(ba[t])); }
#pragma unroll
  for (int c = 0; c < NC; ++c) { bx[c] = xb ^ (c << 5); asm("" : "+v"(bx[c])); }
  auto adr = [&](unsigned soff) {
#pragma unroll
    for (int t = 0; t < 2; ++t) { ca[t] = ba[t] + soff; asm("" : "+v"(ca[t])); }
#pragma unroll
    for (int c = 0; c < NC; ++c) { cx[c] = bx[c] + soff; asm("" : "+v"(cx[c])); }
  };
  auto rdw = [&](unsigned a, int i) { return *(const lds_bf16x8*)(size_t)(a + i * 2048); };
  auto rdx = [&](unsigned a, int t) {
    s16x4 lo = __builtin_amdgcn_ds_read_tr16_b64_v4i16((lds_s16x4*)(size_t)(a + t * 4096));
    s16x4 hi = __builtin_amdgcn_ds_read_tr16_b64_v4i16((lds_s16x4*)(size_t)(a + t * 4096 + 512));
    return __builtin_shufflevector(lo, hi, 0, 1, 2, 3, 4, 5, 6, 7);
  };
  bf16x8 wf[2][4], xf[2][NC];
  auto rd = [&](int t, bool fence = false) {
#pragma unroll
    for (int c = 0; c < 4; ++c) {
      wf[t][c] = rdw(ca[t], c);
      if (fence) SCHED0();
      if (c < NC) xf[t][c] = rdx(cx[c], t);
      if (fence) SCHED0();
    }
  };
  auto mm = [&](int t, int s0 = 0, int s1 = 4) {
#pragma unroll
    for (int s = s0; s < s1; ++s)
#pragma unroll
      for (int u = 0; u < 2 * s + 1; ++u) {
        const int i = u < s ? s : (u < 2 * s ? u - s : s), c = u < s ? u : s;
        if (c < NC) acc[i][c] = __builtin_amdgcn_mfma_f32_16x16x32_bf16(xf[t][c], wf[t][i], acc[i][c], 0, 0, 0);
      }
  };
  auto il = [&]() {
#pragma unroll
    for (int j = 0; j < 4 + 2 * NC; ++j) { SGB(0x8, 1); SGB(0x100, 1); }
#pragma unroll
    for (int j = 4 + 2 * NC; j < 4 * NC; ++j) { SGB(0x8, 1); SGB(0x2, 2); }
  };
  unsigned po[4];
#pragma unroll
  for (int i = 0; i < 4; ++i) { po[i] = poff + (kb0 << 10) + i * PBLK; asm volatile("" : "+v"(po[i])); }
  unsigned sga = lds0 + SIG_OFF + 16 + wid * 4;
  asm volatile("" : "+v"(sga));
  unsigned so = 0;
  adr(so);
  __builtin_amdgcn_s_waitcnt(0xC07F);
  BAR();
  SCHED0();
  rd(0, true);
  SCHED0();
  asm volatile(".p2alignl 6, 0xBF800000\n\t.fill %0, 4, 0xBF800000" ::"n"(4));
  SCHED0();
#pragma unroll 1
  for (int kt = 0; kt < NK - 3; ++kt) {
    so += STAGE_B; if (so == NST * STAGE_B) so = 0;
    SCHED0();
    rd(1); adr(so); mm(0); il(); SCHED0();
    BAR(); SCHED0();
    if (NJ == 1 && kt == WDPF_STEP) {
      asm volatile("buffer_load_dword v231, %0, %1, 0 offen\n\tbuffer_load_dword v231, %0, %1, %2 offen\n\tbuffer_load_dword v231, %0, %1, %3 offen\n\tbuffer_load_dword v231, %0, %1, %4 offen"
                   ::"v"(pfo), "s"(rsPf), "s"(7 * D * 2), "s"(14 * D * 2), "s"(21 * D * 2) : "memory", "v231");
    }
    SCHED0();
    rd(0); mm(1); il(); SCHED0();
  }
  rd(1); adr(((NK - 2) % NST) * STAGE_B); mm(0); il(); SCHED0();
  BAR(); SCHED0();

  static_assert((NK - 1) % NST == 0, "the last stage sits in slot 0");
  bf16x8 w2[4][4], x2[4][NC];
  auto rW = [&](int s, int i) { w2[s][i] = rdw(s < 2 ? ca[s & 1] : ba[s & 1], i); };
  auto rX = [&](int s, int c) { x2[s][c] = rdx(s < 2 ? cx[c] : bx[c], s & 1); };
  auto rS = [&](int s) { rW(s, 0); rX(s, 0); rX(s, 1); rW(s, 1); rW(s, 2); rW(s, 3); };
  auto mf = [&](int s, int i, int c) { acc[i][c] = __builtin_amdgcn_mfma_f32_16x16x32_bf16(x2[s][c], w2[s][i], acc[i][c], 0, 0, 0); };
  auto mS = [&](int s) {
#pragma unroll
    for (int i = 0; i < 4; ++i) { mf(s, i, 0); mf(s, i, 1); }
  };
  auto st = [&](int i, int j) {
    unsigned a0 = pack2(acc[i][2 * j][0], acc[i][2 * j][1]), a1 = pack2(acc[i][2 * j][2], acc[i][2 * j][3]);
    unsigned b0 = pack2(acc[i][2 * j + 1][0], acc[i][2 * j + 1][1]), b1 = pack2(acc[i][2 * j + 1][2], acc[i][2 * j + 1][3]);
    auto s0 = __builtin_amdgcn_permlane16_swap(a0, b0, false, false);
    auto s1 = __builtin_amdgcn_permlane16_swap(a1, b1, false, false);
    u32x4 v = {s0[0], s1[0], s0[1], s1[1]};
    __builtin_amdgcn_raw_buffer_store_b128(v, rsP, po[i] + j * 1024, 0, 16);
  };
  auto ilr = [&](int nm, int per) {
#pragma unroll
    for (int j = 0; j < nm; ++j) { SGB(0x8, 1); if (per == 1) SGB(0x100, 1); else SGB(0x100, 2); }
  };
  if (NJ == 2) {
#pragma unroll
    for (int s = 0; s < 2; ++s) { rX(s, 0); rW(s, 0); rX(s, 1); }
#pragma unroll
    for (int s = 0; s < 2; ++s) rW(s, 1);
    mm(1, 0, 3); ilr(9, 2);
    SCHED0(); BAR(); SCHED0();
#pragma unroll
    for (int s = 2; s < 4; ++s) { rX(s, 0); rW(s, 0); rX(s, 1); }
#pragma unroll
    for (int s = 2; s < 4; ++s) rW(s, 1);
    mm(1, 3, 4); ilr(7, 2);
    SCHED0();
#pragma unroll
    for (int u = 0; u < 8; ++u) {
      const int i = u & 3, j = u >> 2;
      if (u < 2) {
#pragma unroll
        for (int s = 0; s < 4; ++s) rW(s, u + 2);
        rX(u, 2); rX(u, 3);
      }
      if (u == 2) { rX(2, 2); rX(2, 3); rX(3, 2); rX(3, 3); }
#pragma unroll
      for (int s = 0; s < 4; ++s) { mf(s, i, 2 * j); mf(s, i, 2 * j + 1); }
      if (u > 0) st((u - 1) & 3, (u - 1) >> 2);
      SGB(0x8, 1); SGB(0x100, 2); SGB(0x8, 1); SGB(0x100, 2);
      SGB(0x8, 1); SGB(0x2, 2); SGB(0x8, 1); SGB(0x2, 2); SGB(0x8, 1); SGB(0x2, 2); SGB(0x8, 1); SGB(0x40, 1);
      SGB(0x8, 1); SGB(0x100, 2); SGB(0x8, 1); SGB(0x100, 2);
      SCHED0();

    }
    st(3, NJ - 1);
  } else {
    rS(0); rS(1); mm(1); ilr(8, 2); SCHED0();
    BAR(); SCHED0();
    rS(2); mS(0); ilr(8, 1); SCHED0();
    rS(3); mS(1); ilr(8, 1); SCHED0();
    mS(2); mS(3); SCHED0();
#pragma unroll
    for (int i = 0; i < 4; ++i) st(i, 0);
  }
  SCHED0();
  sig(sga, 1);
}

__device__ __forceinline__ bf16x8 add_bf16x8(u32x4 a, u32x4 b, float one) {
  u32x4 r;
#pragma unroll
  for (int i = 0; i < 4; ++i) {
    float lo = __builtin_fmaf(__builtin_bit_cast(float, a[i] << 16), one, __builtin_bit_cast(float, b[i] << 16));
    float hi = __builtin_fmaf(__builtin_bit_cast(float, a[i] & 0xffff0000u), one, __builtin_bit_cast(float, b[i] & 0xffff0000u));
    asm("" : "+v"(lo));
    r[i] = pack2(lo, hi);
  }
  return __builtin_bit_cast(bf16x8, r);
}

__global__ __launch_bounds__(512, 2) void fnet(const bf16* Wr_, const bf16* Wi_, const bf16* x_, const bf16* Dr_, const bf16* Di_, bf16* y_, char* ws_) {
  extern __shared__ __attribute__((aligned(16))) char smem[];
  typedef const volatile __attribute__((address_space(4))) unsigned long long karg_t;
  karg_t* const kp = (karg_t*)__builtin_amdgcn_kernarg_segment_ptr();
  const char* const Wr = (const char*)kp[0];
  const char* const Wi = (const char*)kp[1];
  const char* const x = (const char*)kp[2];
  const char* const Dr = (const char*)kp[3];
  const char* const Di = (const char*)kp[4];
  char* const y = (char*)kp[5];
  char* const ws = (char*)kp[6];
  unsigned long long pc0;
  asm volatile("s_getpc_b64 %0" : "=s"(pc0));
  int b = blockIdx.x, tid = threadIdx.x;
  asm volatile("" : "+s"(b));
  asm volatile("" : "+v"(tid));
  SCHED0();
  const int xcd = b & 7, idx = b >> 3;
  const int mem = idx & 7, nt = mem & 3, ks = mem >> 2, tbl = idx >> 3, tb = xcd * 4 + tbl;
  const int lane = tid & 63, wid = __builtin_amdgcn_readfirstlane(tid >> 6);
  const unsigned lds0 = (unsigned)(size_t)smem;
  if (wid == 5) {
    asm volatile("buffer_load_dword v231, %0, %1, 0 offen" ::"v"((lane * 32 + idx) * 128), "s"(MKRS(pc0 & ~127ull, CODE_PF_BYTES)) : "memory", "v231");
  }
  SCHED0();
  const int lw = wid & 3, r5 = lw * 8 + (lane >> 3);
  unsigned srcA = ((tb * 64 + r5) * S + ks * KLEN + (((lane & 7) ^ ((r5 >> 1) & 7)) << 3)) * 2;
  unsigned srcB = ((ks * KLEN + r5) * D + nt * 128 + (((lane & 7) ^ ((r5 & 2) | ((r5 >> 1) & 4))) << 3)) * 2;
  unsigned ldw = lds0 + lw * 1024, k0 = 4 * nt;
  unsigned srcD = ((nt * 112 + r5) * D + (((lane & 7) ^ ((r5 >> 1) & 7)) << 3)) * 2;
  asm volatile("" : "+v"(srcA), "+v"(srcB), "+v"(srcD));
  asm volatile("" : "+s"(ldw), "+s"(k0));
  SCHED0();
  if (wid == 4) {
    asm volatile("buffer_load_dword v231, %0, %1, 0 offen" ::"v"(lane < 56 ? b * (8 * D * 2) + lane * 128 : 0x7ffffff0u), "s"(MKRS(x, S * D * 2)) : "memory", "v231");
  }
  SCHED0();
  const int et = nt, th = ks;
  unsigned long long ep = 0;
  const int par = wid >> 2, im = (wid >> 1) & 1, tt = wid & 1, kg = im * 2 + par;
  unsigned dbase;
  auto mk_dbase = [&]() {
    int l = lane;
    asm volatile("" : "+v"(l));
    dbase = lds0 + (l & 15) * 128 + (((l >> 4) ^ ((l & 15) >> 1) ^ (par << 2)) << 4) + im * 16384;
  };
  bf16x8 de[4][7];
  const unsigned sgL = lds0 + SIG_OFF, sgC = sgL + 16;

  if (__builtin_expect(wid < 4, 1)) {
    const rsrc_t rsR = MKRS(Wr, S * S * 2), rsI = MKRS(Wi, S * S * 2), rsX = MKRS(x, S * D * 2);
#define WAIT_S(ns) do { if (nt == 3) WAIT_V(6 * (ns)); else WAIT_V(8 * (ns)); } while (0)
    auto stW = [&](int p, unsigned slot) {
      const unsigned sw = ((p + k0) & 15) * 128, dst = ldw + slot;
#pragma unroll
      for (int i = 0; i < 2; ++i) dma16(rsR, srcA, sw + i * (32 * S * 2), dst + i * 4096);
#pragma unroll
      for (int i = 0; i < 2; ++i) dma16(rsI, srcA, sw + i * (32 * S * 2), dst + 8192 + i * 4096);
    };
    auto stX = [&](int p, unsigned slot) {
      const unsigned sx = ((p + k0) & 15) * (64 * D * 2), dst = ldw + slot;
#pragma unroll
      for (int i = 0; i < 2; ++i) dma16(rsX, srcB, sx + i * (32 * D * 2), dst + A_B + i * 4096);
      if (__builtin_expect(nt < 3, 1)) {
#pragma unroll
        for (int i = 0; i < 2; ++i) dma16(rsX, srcB, sx + 128 + i * (32 * D * 2), dst + A_B + 8192 + i * 4096);
      }
    };
    unsigned slot = 0;
#pragma unroll 1
    for (int p = 0; p < NK; ++p) {
      stW(p, slot); stX(p, slot);
      slot += STAGE_B; if (slot == NST * STAGE_B) slot = 0;
      if (__builtin_expect(p >= 5, 1)) { WAIT_V(12); BAR(); }
      else if (p & 3) { WAIT_S(1); BAR(); }
      else if (p == 0) {
        int w = lw, t = tb;
        asm volatile("" : "+s"(w), "+s"(t));
        const unsigned o0 = (w * 128 + t * 4) * PBLK + 14 * 1024, od = (et * 112 + w * 28) * (D * 2);
        asm volatile("s_load_dword s100, %0, %4\n\ts_load_dword s100, %0, %5\n\ts_load_dword s100, %1, %6\n\ts_load_dword s100, %2, %7\n\ts_load_dword s100, %3, %7\n\ts_load_dword s100, %0, %8"
                     ::"s"(ws), "s"(y), "s"(Dr), "s"(Di), "s"(o0), "s"(o0 + 3 * PBLK), "s"((t * 64 + w * 16) * (D * 2)), "s"(od), "s"(SYNC_OFF + t * 256 + 64) : "s100");
      }
    }
    const rsrc_t rsDr = MKRS(Dr, D * D * 2), rsDi = MKRS(Di, D * D * 2);
    const rsrc_t rsD3 = lw < 2 ? rsDr : rsDi;
    const unsigned srcD3 = lw < 2 ? srcD + 96 * D * 2 : srcD - 16 * D * 2;
    const unsigned sgw = sgL + wid * 4;
    if (tid < 16) *(lds_u32*)(size_t)(sgL + tid * 4) = 0;
    auto unit = [&](int u, int sl) {
      const unsigned so = u * 128, dst = ldw + sl * STAGE_B;
#pragma unroll
      for (int i = 0; i < 3; ++i) dma16(rsDr, srcD, so + i * (32 * D * 2), dst + i * 4096);
      dma16(rsD3, srcD3, so, dst + (lw < 2 ? 3 * 4096 : 16384 - 2048));
#pragma unroll
      for (int i = 0; i < 3; ++i) dma16(rsDi, srcD, so + (16 + i * 32) * (D * 2), dst + 16384 + 2048 + i * 4096);
    };
#pragma unroll 1
    for (int u = 0; u < 2; ++u) {
      unit(u, u + 1);
      if (u == 0) { WAIT_V(UN + 6); WAIT_L(0); BAR(); WAIT_V(UN); BAR(); }
    }
    mk_dbase();
    WAIT_V(0); sig(sgw, 1);
    wait_sig(sgL, 1);
    rdD(de[0], dbase, 0);
    rdD(de[1], dbase, 1);
    sig(sgw, 2);
    wait_sig(sgC, 1);
#pragma unroll 1
    for (int u = 2; u < 7; ++u) {
      if (u == 5) { wait_sig(sgC, 2); wait_sig(sgL, 2); }
      unit(u, u >= 4 ? u - 4 : u + 1);
      if (u == 5) { WAIT_V(2 * UN); sig(sgw, 3); }
    }
  } else {
    const int cw = wid & 3;
    mk_dbase();
    const rsrc_t rsP = MKRS(ws, PART_BYTES);
    const int wm = cw >> 1, wn = cw & 1;
    const unsigned poff = (ks * 256 + wm * 128 + tb * 4) * PBLK + (((lane >> 5) | ((lane >> 3) & 2)) * 16 + (lane & 15)) * 16;
    const unsigned pfo = lane < 49 ? (((tbl * 2 + ks) * 2 + wn) * 28) * (D * 2) + lane * 128 : 0x7ffffff0u;
    if (nt < 3) p1_compute<2>(lds0, cw, lane, nt * 4 + wn * 2, poff, rsP, rsP, pfo);
    else p1_compute<1>(lds0, cw, lane, 12 + wn, poff, rsP, MKRS(wm ? Di : Dr, D * D * 2), pfo);
    ep = *(const volatile __attribute__((address_space(4))) unsigned long long*)(ws + SYNC_OFF + tb * 256 + 64) + 1;
    wait_sig(sgL, 1);
    rdD(de[0], dbase, 0);
    rdD(de[1], dbase, 1);
    sig(sgL + wid * 4, 2);
    WAIT_V(0);
    if (lane == 0) {
      unsigned old = __hip_atomic_fetch_add((lds_u32*)(size_t)(lds0 + CNT_OFF), 1u, __ATOMIC_RELAXED, __HIP_MEMORY_SCOPE_WORKGROUP);
      if (old == 3) __hip_atomic_store((gu64*)(ws + SYNC_OFF) + tb * 32 + mem, ep, RLX_AGENT);
    }
  }
  gu64* const flags = (gu64*)(ws + SYNC_OFF) + tb * 32;
  wait_sig(sgL, 3);
  unsigned long long f0 = ep;
  if (wid == 4) f0 = __hip_atomic_load(flags + (lane & 7), RLX_AGENT);
  SCHED0();
  rdD(de[2], dbase, 2);
  rdD(de[3], dbase, 3);
  SCHED0();
  if (wid == 4) {
#pragma unroll 1
    for (unsigned spins = 0; spins < (1u << 22); ++spins) {
      if (__all(f0 == ep)) break;
      f0 = __hip_atomic_load(flags + (lane & 7), RLX_AGENT);
    }
  }
  if (wid < 4) WAIT_V(0);
  WAIT_L(0);
  const rsrc_t rsP = MKRS(ws, PART_BYTES);
  unsigned pbase = (im * 128 + tb * 4 + th * 2 + tt) * PBLK + par * 1024 + lane * 16;
  asm volatile("" : "+v"(pbase));
  SCHED0();
  BAR();
  asm volatile("" ::: "memory");
  u32x4 pf[7][2];
  auto ldP = [&](int n) {
#pragma unroll
    for (int s = 0; s < 2; ++s)
      pf[n][s] = __builtin_amdgcn_raw_buffer_load_b128(rsP, pbase, n * 2048 + s * (256 * PBLK), 16);
    SCHED0();
  };
#pragma unroll
  for (int n = 0; n < PB0; ++n) ldP(n);
  SCHED0();
  f32x4 ya[7];
#pragma unroll
  for (int c = 0; c < 7; ++c) ya[c] = f32x4{0.f, 0.f, 0.f, 0.f};
  float one = 1.0f;
  asm volatile("" : "+s"(one));
  int tl = tid;
  asm volatile("" : "+v"(tl));
  const int fr = tl & 15, g = (tl >> 4) & 3;
  unsigned wa[4];
#pragma unroll
  for (int c = 0; c < 4; ++c) {
    wa[c] = lds0 + RED_OFF + (kg * 32 + tt * 16 + fr) * 512 + (g & 1) * 256 + (((2 * c + (g >> 1)) ^ (fr & 7)) << 4);
    asm volatile("" : "+v"(wa[c]));
  }
  unsigned rb = lds0 + RED_OFF + (tl >> 4) * 512 + ((fr ^ ((tl >> 4) & 7)) << 4);
  unsigned yo = fr < 14 ? ((tb * 64 + th * 32 + (tl >> 4)) * D + et * 112 + fr * 8) * 2 : 0x7ffffff0u;
  asm volatile("" : "+v"(rb), "+v"(yo));
  SCHED0();
  bf16x8 ps = add_bf16x8(pf[0][0], pf[0][1], one);
  SCHED0();
#pragma unroll
  for (int n = 0; n < 7; ++n) {
    if (PB0 + n < 7) ldP(PB0 + n);
    if (n == 6 && tid == 256 && mem == 0) __hip_atomic_store(flags + 8, ep, RLX_AGENT);
    SCHED0();
    bf16x8 pn = ps;
    if (n < 6) pn = add_bf16x8(pf[n + 1][0], pf[n + 1][1], one);
#pragma unroll
    for (int c = 0; c < 7; ++c) ya[c] = __builtin_amdgcn_mfma_f32_16x16x32_bf16(de[n & 3][c], ps, ya[c], 0, 0, 0);
    if (n < 6) {
#pragma unroll
      for (int c = 0; c < 7; ++c) {
        __builtin_amdgcn_sched_group_barrier(0x8, 1, 0);
        __builtin_amdgcn_sched_group_barrier(0x2, 4, 0);
      }
    } else {
#pragma unroll
      for (int c = 0; c < 7; ++c) *(lds_f32x4*)(size_t)(wa[c & 3] + (c >> 2) * 128) = ya[c];
      __builtin_amdgcn_sched_group_barrier(0x8, 3, 0);
#pragma unroll
      for (int c = 0; c < 4; ++c) {
        __builtin_amdgcn_sched_group_barrier(0x200, 1, 0);
        __builtin_amdgcn_sched_group_barrier(0x8, 1, 0);
      }
      __builtin_amdgcn_sched_group_barrier(0x200, 3, 0);
    }
    SCHED0();
    if (n < 3) rdD(de[n], dbase, n + 4);
    SCHED0();
    ps = pn;
  }
  WAIT_L(0);
  BAR();
  {
    f32x4 r[2][4], o[2];
#pragma unroll
    for (int z = 0; z < 2; ++z)
#pragma unroll
      for (int k = 0; k < 4; ++k) r[z][k] = *(const lds_f32x4*)(size_t)(rb + z * 256 + k * (32 * 512));
    SCHED0();
#pragma unroll
    for (int z = 0; z < 2; ++z) o[z] = (r[z][0] + r[z][1]) - (r[z][2] + r[z][3]);
    u32x4 v = {pack2(o[0][0], o[0][1]), pack2(o[0][2], o[0][3]), pack2(o[1][0], o[1][1]), pack2(o[1][2], o[1][3])};
    __builtin_amdgcn_raw_buffer_store_b128(v, MKRS(y, S * D * 2), yo, 0, 16);
  }
}

extern "C" void kernel_launch(void* const* d_in, const int* in_sizes, int n_in, void* d_out, int out_size,
                              void* d_ws, size_t ws_size, hipStream_t stream) {
  const bf16* x = (const bf16*)d_in[0];
  const bf16* Wr = (const bf16*)d_in[1];
  const bf16* Wi = (const bf16*)d_in[2];
  const bf16* Dr = (const bf16*)d_in[3];
  const bf16* Di = (const bf16*)d_in[4];
  (void)hipFuncSetAttribute((const void*)fnet, hipFuncAttributeMaxDynamicSharedMemorySize, LDS_BYTES);
  fnet<<<256, 512, LDS_BYTES, stream>>>(Wr, Wi, x, Dr, Di, (bf16*)d_out, (char*)d_ws);
}
```

```cpp
#include <hip/hip_runtime.h>
#include <hip/hip_bf16.h>
#include <stdint.h>
typedef __hip_bfloat16 bf16;
typedef __attribute__((ext_vector_type(8))) short bf16x8;
typedef __attribute__((ext_vector_type(4))) short s16x4;
typedef __attribute__((ext_vector_type(4))) float f32x4;
typedef __attribute__((ext_vector_type(4))) unsigned u32x4;
typedef __attribute__((ext_vector_type(2))) float f32x2;
typedef __attribute__((ext_vector_type(2))) __bf16 bf16x2_t;
typedef __attribute__((address_space(3))) s16x4 lds_s16x4;
typedef __attribute__((address_space(3))) bf16x8 lds_bf16x8;
typedef __attribute__((address_space(3))) f32x4 lds_f32x4;
typedef __attribute__((address_space(3))) unsigned lds_u32;
typedef __attribute__((address_space(3))) u32x4 lds_u32x4;
typedef __attribute__((address_space(1))) unsigned long long gu64;
typedef __amdgpu_buffer_rsrc_t rsrc_t;
#define RLX_AGENT __ATOMIC_RELAXED, __HIP_MEMORY_SCOPE_AGENT

constexpr int S = 2048, D = 448;
#define WAIT_V(n) asm volatile("s_waitcnt vmcnt(%0)" ::"n"(n) : "memory")
#define WAIT_L(n) asm volatile("s_waitcnt lgkmcnt(%0)" ::"n"(n) : "memory")
#define SCHED0() __builtin_amdgcn_sched_barrier(0)
#define BAR() __builtin_amdgcn_s_barrier()
#define MKRS(p, bytes) __builtin_amdgcn_make_buffer_rsrc((void*)(p), 0, (bytes), 0x00020000)

constexpr int WDPF_STEP = 3;
#define PRIO(n) do { __builtin_amdgcn_s_setprio(n); SCHED0(); } while (0)
constexpr int PB0 = 3;
constexpr int CODE_PF_BYTES = 11164;

__device__ __forceinline__ unsigned pack2(float a, float b) {
  bf16x2_t v = __builtin_convertvector(f32x2{a, b}, bf16x2_t);
  return __builtin_bit_cast(unsigned, v);
}

__device__ __forceinline__ void dma16(rsrc_t rs, unsigned voff, unsigned soff, unsigned lds) {
  asm volatile("s_mov_b32 m0, %0\n\ts_nop 0\n\tbuffer_load_dwordx4 %1, %2, %3 offen lds" ::"s"(lds), "v"(voff), "s"(rs), "s"(soff) : "memory");
}

constexpr int KS = 2, KLEN = S / KS, NK = KLEN / 64;
constexpr int A_B = 128 * 128, STAGE_B = 32768;
constexpr int LDS_BYTES = 160 * 1024;
constexpr int PBLK = 14 * 1024 + 128;
constexpr int PART_BYTES = KS * 256 * PBLK;
constexpr int SYNC_OFF = 16 << 20;
constexpr unsigned long long EPOCH_STEP = 0x9E3779B97F4A7C15ull;
constexpr int NST = 5;
constexpr int UN = 7;
__host__ __device__ constexpr int unit_slot(int u) { return (u + 1) % 5; }
constexpr int RED_OFF = 3 * STAGE_B;
constexpr int SIG_OFF = STAGE_B + 15360, CNT_OFF = SIG_OFF + 32;

__device__ __forceinline__ void sig(unsigned addr, unsigned val) {
  asm volatile("s_mov_b64 exec, 1\n\tds_write_b32 %0, %1\n\ts_mov_b64 exec, -1" ::"v"(addr), "v"(val) : "memory");
}
__device__ __forceinline__ void wait_sig(unsigned addr, unsigned target) {
#pragma unroll 1
  for (unsigned spins = 0; spins < (1u << 24); ++spins) {
    const u32x4 v = *(const volatile lds_u32x4*)(size_t)addr;
    const unsigned m = min(min(v[0], v[1]), min(v[2], v[3]));
    if (__builtin_amdgcn_readfirstlane(m) >= target) break;
    __builtin_amdgcn_s_sleep(1);
  }
  asm volatile("" ::: "memory");
}

__device__ __forceinline__ void rdD(bf16x8* d, unsigned dbase, int n) {
  unsigned eb = dbase + unit_slot(n) * STAGE_B;
  if (unit_slot(n) >= 2) asm("" : "+v"(eb));
#pragma unroll
  for (int c = 0; c < 7; ++c) d[c] = *(const lds_bf16x8*)(size_t)(eb + c * 2048);
}

#define SGB(mask, n) __builtin_amdgcn_sched_group_barrier(mask, n, 0)
template <int NJ>
__device__ __forceinline__ void p1_compute(unsigned lds0, int wid, int lane, int kb0, unsigned poff, rsrc_t rsP, rsrc_t rsPf, unsigned pfo, gu64* epp, unsigned long long& ep) {
  constexpr int NC = 2 * NJ;
  f32x4 acc[4][NC];
#pragma unroll
  for (int i = 0; i < 4; ++i)
#pragma unroll
    for (int c = 0; c < NC; ++c) { acc[i][c] = f32x4{0.f, 0.f, 0.f, 0.f}; asm volatile("" : "+v"(acc[i][c])); }
  const int wm = wid >> 1, wn = wid & 1;
  const int fr = lane & 15, g = lane >> 4, q = (lane >> 2) & 3, p = lane & 3;
  const unsigned ab = lds0 + (wm * 64 + fr) * 128 + ((g ^ (fr >> 1)) << 4);
  unsigned xb = lds0 + A_B + (8 * g + q) * 128 + (p & 1) * 8 + (((p >> 1) ^ (q & 2) ^ ((g & 1) << 2)) << 4);
  if (NJ == 2) xb += wn * 8192; else xb ^= wn << 6;
  unsigned ba[2], bx[NC], ca[2], cx[NC];
#pragma unroll
  for (int t = 0; t < 2; ++t) { ba[t] = ab ^ (t << 6); asm("" : "+v"(ba[t])); }
#pragma unroll
  for (int c = 0; c < NC; ++c) { bx[c] = xb ^ (c << 5); asm("" : "+v"(bx[c])); }
  auto adr = [&](unsigned soff) {
#pragma unroll
    for (int t = 0; t < 2; ++t) { ca[t] = ba[t] + soff; asm("" : "+v"(ca[t])); }
#pragma unroll
    for (int c = 0; c < NC; ++c) { cx[c] = bx[c] + soff; asm("" : "+v"(cx[c])); }
  };
  auto rdw = [&](unsigned a, int i) { return *(const lds_bf16x8*)(size_t)(a + i * 2048); };
  auto rdx = [&](unsigned a, int t) {
    s16x4 lo = __builtin_amdgcn_ds_read_tr16_b64_v4i16((lds_s16x4*)(size_t)(a + t * 4096));
    s16x4 hi = __builtin_amdgcn_ds_read_tr16_b64_v4i16((lds_s16x4*)(size_t)(a + t * 4096 + 512));
    return __builtin_shufflevector(lo, hi, 0, 1, 2, 3, 4, 5, 6, 7);
  };
  bf16x8 wf[2][4], xf[2][NC];
  auto rd = [&](int t, bool fence = false) {
#pragma unroll
    for (int c = 0; c < 4; ++c) {
      wf[t][c] = rdw(ca[t], c);
      if (fence) SCHED0();
      if (c < NC) xf[t][c] = rdx(cx[c], t);
      if (fence) SCHED0();
    }
  };
  auto mm = [&](int t, int s0 = 0, int s1 = 4) {
#pragma unroll
    for (int s = s0; s < s1; ++s)
#pragma unroll
      for (int u = 0; u < 2 * s + 1; ++u) {
        const int i = u < s ? s : (u < 2 * s ? u - s : s), c = u < s ? u : s;
        if (c < NC) acc[i][c] = __builtin_amdgcn_mfma_f32_16x16x32_bf16(xf[t][c], wf[t][i], acc[i][c], 0, 0, 0);
      }
  };
  auto il = [&]() {
#pragma unroll
    for (int j = 0; j < 4 + 2 * NC; ++j) { SGB(0x8, 1); SGB(0x100, 1); }
#pragma unroll
    for (int j = 4 + 2 * NC; j < 4 * NC; ++j) { SGB(0x8, 1); SGB(0x2, 2); }
  };
  unsigned po[4];
#pragma unroll
  for (int i = 0; i < 4; ++i) { po[i] = poff + (kb0 << 10) + i * PBLK; asm volatile("" : "+v"(po[i])); }
  unsigned sga = lds0 + SIG_OFF + 16 + wid * 4;
  asm volatile("" : "+v"(sga));
  unsigned so = 0;
  adr(so);
  __builtin_amdgcn_s_waitcnt(0xC07F);
  BAR();
  SCHED0();
  rd(0, true);
  SCHED0();
  asm volatile(".p2alignl 6, 0xBF800000\n\t.fill %0, 4, 0xBF800000" ::"n"(4));
  SCHED0();
#pragma unroll 1
  for (int kt = 0; kt < NK - 3; ++kt) {
    so += STAGE_B; if (so == NST * STAGE_B) so = 0;
    SCHED0();
    rd(1); adr(so); mm(0); il(); SCHED0();
    BAR(); SCHED0();
    if (NJ == 1 && kt == WDPF_STEP) {
      asm volatile("buffer_load_dword v231, %0, %1, 0 offen\n\tbuffer_load_dword v231, %0, %1, %2 offen\n\tbuffer_load_dword v231, %0, %1, %3 offen\n\tbuffer_load_dword v231, %0, %1, %4 offen"
                   ::"v"(pfo), "s"(rsPf), "s"(7 * D * 2), "s"(14 * D * 2), "s"(21 * D * 2) : "memory", "v231");
    }
    SCHED0();
    rd(0); mm(1); il(); SCHED0();
  }
  rd(1); adr(((NK - 2) % NST) * STAGE_B); mm(0); il(); SCHED0();
  BAR(); SCHED0();
  ep = __hip_atomic_load(epp, RLX_AGENT);
  SCHED0();

  static_assert((NK - 1) % NST == 0, "the last stage sits in slot 0");
  bf16x8 w2[4][4], x2[4][NC];
  auto rW = [&](int s, int i) { w2[s][i] = rdw(s < 2 ? ca[s & 1] : ba[s & 1], i); };
  auto rX = [&](int s, int c) { x2[s][c] = rdx(s < 2 ? cx[c] : bx[c], s & 1); };
  auto rS = [&](int s) { rW(s, 0); rX(s, 0); rX(s, 1); rW(s, 1); rW(s, 2); rW(s, 3); };
  auto mf = [&](int s, int i, int c) { acc[i][c] = __builtin_amdgcn_mfma_f32_16x16x32_bf16(x2[s][c], w2[s][i], acc[i][c], 0, 0, 0); };
  auto mS = [&](int s) {
#pragma unroll
    for (int i = 0; i < 4; ++i) { mf(s, i, 0); mf(s, i, 1); }
  };
  auto st = [&](int i, int j) {
    unsigned a0 = pack2(acc[i][2 * j][0], acc[i][2 * j][1]), a1 = pack2(acc[i][2 * j][2], acc[i][2 * j][3]);
    unsigned b0 = pack2(acc[i][2 * j + 1][0], acc[i][2 * j + 1][1]), b1 = pack2(acc[i][2 * j + 1][2], acc[i][2 * j + 1][3]);
    auto s0 = __builtin_amdgcn_permlane16_swap(a0, b0, false, false);
    auto s1 = __builtin_amdgcn_permlane16_swap(a1, b1, false, false);
    u32x4 v = {s0[0], s1[0], s0[1], s1[1]};
    __builtin_amdgcn_raw_buffer_store_b128(v, rsP, po[i] + j * 1024, 0, 16);
  };
  auto ilr = [&](int nm, int per) {
#pragma unroll
    for (int j = 0; j < nm; ++j) { SGB(0x8, 1); if (per == 1) SGB(0x100, 1); else SGB(0x100, 2); }
  };
  if (NJ == 2) {
#pragma unroll
    for (int s = 0; s < 2; ++s) { rX(s, 0); rW(s, 0); rX(s, 1); }
#pragma unroll
    for (int s = 0; s < 2; ++s) rW(s, 1);
    mm(1, 0, 3); ilr(9, 2);
    SCHED0(); BAR(); SCHED0();
#pragma unroll
    for (int s = 2; s < 4; ++s) { rX(s, 0); rW(s, 0); rX(s, 1); }
#pragma unroll
    for (int s = 2; s < 4; ++s) rW(s, 1);
    mm(1, 3, 4); ilr(7, 2);
    SCHED0();
#pragma unroll
    for (int u = 0; u < 8; ++u) {
      const int i = u & 3, j = u >> 2;
      if (u < 2) {
#pragma unroll
        for (int s = 0; s < 4; ++s) rW(s, u + 2);
        rX(u, 2); rX(u, 3);
      }
      if (u == 2) { rX(2, 2); rX(2, 3); rX(3, 2); rX(3, 3); }
#pragma unroll
      for (int s = 0; s < 4; ++s) { mf(s, i, 2 * j); mf(s, i, 2 * j + 1); }
      if (u > 0) st((u - 1) & 3, (u - 1) >> 2);
      SGB(0x8, 1); SGB(0x100, 2); SGB(0x8, 1); SGB(0x100, 2);
      SGB(0x8, 1); SGB(0x2, 2); SGB(0x8, 1); SGB(0x2, 2); SGB(0x8, 1); SGB(0x2, 2); SGB(0x8, 1); SGB(0x40, 1);
      SGB(0x8, 1); SGB(0x100, 2); SGB(0x8, 1); SGB(0x100, 2);
      SCHED0();

    }
    st(3, NJ - 1);
  } else {
    rS(0); rS(1); mm(1); ilr(8, 2); SCHED0();
    BAR(); SCHED0();
    rS(2); mS(0); ilr(8, 1); SCHED0();
    rS(3); mS(1); ilr(8, 1); SCHED0();
    mS(2); mS(3); SCHED0();
#pragma unroll
    for (int i = 0; i < 4; ++i) st(i, 0);
  }
  SCHED0();
  sig(sga, 1);
}

__device__ __forceinline__ bf16x8 add_bf16x8(u32x4 a, u32x4 b, float one) {
  u32x4 r;
#pragma unroll
  for (int i = 0; i < 4; ++i) {
    float lo = __builtin_fmaf(__builtin_bit_cast(float, a[i] << 16), one, __builtin_bit_cast(float, b[i] << 16));
    float hi = __builtin_fmaf(__builtin_bit_cast(float, a[i] & 0xffff0000u), one, __builtin_bit_cast(float, b[i] & 0xffff0000u));
    asm("" : "+v"(lo));
    r[i] = pack2(lo, hi);
  }
  return __builtin_bit_cast(bf16x8, r);
}

__global__ __launch_bounds__(512, 2) void fnet(const bf16* Wr_, const bf16* Wi_, const bf16* x_, const bf16* Dr_, const bf16* Di_, bf16* y_, char* ws_) {
  extern __shared__ __attribute__((aligned(16))) char smem[];
  typedef const volatile __attribute__((address_space(4))) unsigned long long karg_t;
  karg_t* const kp = (karg_t*)__builtin_amdgcn_kernarg_segment_ptr();
  const char* const Wr = (const char*)kp[0];
  const char* const Wi = (const char*)kp[1];
  const char* const x = (const char*)kp[2];
  const char* const Dr = (const char*)kp[3];
  const char* const Di = (const char*)kp[4];
  char* const y = (char*)kp[5];
  char* const ws = (char*)kp[6];
  unsigned long long pc0;
  asm volatile("s_getpc_b64 %0" : "=s"(pc0));
  int b = blockIdx.x, tid = threadIdx.x;
  asm volatile("" : "+s"(b));
  asm volatile("" : "+v"(tid));
  SCHED0();
  const int xcd = b & 7, idx = b >> 3;
  const int mem = idx & 7, nt = mem & 3, ks = mem >> 2, tbl = idx >> 3, tb = xcd * 4 + tbl;
  const int lane = tid & 63, wid = __builtin_amdgcn_readfirstlane(tid >> 6);
  const unsigned lds0 = (unsigned)(size_t)smem;
  if (wid >= 5) {
    const unsigned o = ((wid - 5) * 32 + idx) * 128 + 60;
    if (o + 8 <= CODE_PF_BYTES) asm volatile("s_load_dwordx2 s[100:101], %0, %1" ::"s"(pc0 & ~127ull), "s"(o) : "s100", "s101");
  }
  SCHED0();
  const int lw = wid & 3, r5 = lw * 8 + (lane >> 3);
  unsigned srcA = ((tb * 64 + r5) * S + ks * KLEN + (((lane & 7) ^ ((r5 >> 1) & 7)) << 3)) * 2;
  unsigned srcB = ((ks * KLEN + r5) * D + nt * 128 + (((lane & 7) ^ ((r5 & 2) | ((r5 >> 1) & 4))) << 3)) * 2;
  unsigned ldw = lds0 + lw * 1024, k0 = 4 * nt;
  unsigned srcD = ((nt * 112 + r5) * D + (((lane & 7) ^ ((r5 >> 1) & 7)) << 3)) * 2;
  asm volatile("" : "+v"(srcA), "+v"(srcB), "+v"(srcD));
  asm volatile("" : "+s"(ldw), "+s"(k0));
  SCHED0();
  if (wid == 4) {
    asm volatile("buffer_load_dword v231, %0, %1, 0 offen" ::"v"(lane < 56 ? b * (8 * D * 2) + lane * 128 : 0x7ffffff0u), "s"(MKRS(x, S * D * 2)) : "memory", "v231");
  }
  SCHED0();
  const int et = nt, th = ks;
  unsigned long long ep = 0;
  const int par = wid >> 2, im = (wid >> 1) & 1, tt = wid & 1, kg = im * 2 + par;
  unsigned dbase;
  auto mk_dbase = [&]() {
    int l = lane;
    asm volatile("" : "+v"(l));
    dbase = lds0 + (l & 15) * 128 + (((l >> 4) ^ ((l & 15) >> 1) ^ (par << 2)) << 4) + im * 16384;
  };
  bf16x8 de[4][7];
  const unsigned sgL = lds0 + SIG_OFF, sgC = sgL + 16;

  if (__builtin_expect(wid < 4, 1)) {
    const rsrc_t rsR = MKRS(Wr, S * S * 2), rsI = MKRS(Wi, S * S * 2), rsX = MKRS(x, S * D * 2);
#define WAIT_S(ns) do { if (nt == 3) WAIT_V(6 * (ns)); else WAIT_V(8 * (ns)); } while (0)
    auto stW = [&](int p, unsigned slot) {
      const unsigned sw = ((p + k0) & 15) * 128, dst = ldw + slot;
#pragma unroll
      for (int i = 0; i < 2; ++i) dma16(rsR, srcA, sw + i * (32 * S * 2), dst + i * 4096);
#pragma unroll
      for (int i = 0; i < 2; ++i) dma16(rsI, srcA, sw + i * (32 * S * 2), dst + 8192 + i * 4096);
    };
    auto stX = [&](int p, unsigned slot) {
      const unsigned sx = ((p + k0) & 15) * (64 * D * 2), dst = ldw + slot;
#pragma unroll
      for (int i = 0; i < 2; ++i) dma16(rsX, srcB, sx + i * (32 * D * 2), dst + A_B + i * 4096);
      if (__builtin_expect(nt < 3, 1)) {
#pragma unroll
        for (int i = 0; i < 2; ++i) dma16(rsX, srcB, sx + 128 + i * (32 * D * 2), dst + A_B + 8192 + i * 4096);
      }
    };
    unsigned slot = 0;
#pragma unroll 1
    for (int p = 0; p < NK; ++p) {
      stW(p, slot); stX(p, slot);
      slot += STAGE_B; if (slot == NST * STAGE_B) slot = 0;
      if (__builtin_expect(p >= 5, 1)) { WAIT_V(12); BAR(); }
      else if (p & 3) { WAIT_S(1); BAR(); }
      else if (p == 0) {
        int w = lw, t = tb;
        asm volatile("" : "+s"(w), "+s"(t));
        const unsigned o0 = (w * 128 + t * 4) * PBLK + 14 * 1024, od = (et * 112 + w * 28) * (D * 2);
        asm volatile("s_load_dword s100, %0, %4\n\ts_load_dword s100, %0, %5\n\ts_load_dword s100, %1, %6\n\ts_load_dword s100, %2, %7\n\ts_load_dword s100, %3, %7\n\ts_load_dword s100, %0, %8"
                     ::"s"(ws), "s"(y), "s"(Dr), "s"(Di), "s"(o0), "s"(o0 + 3 * PBLK), "s"((t * 64 + w * 16) * (D * 2)), "s"(od), "s"(SYNC_OFF + t * 256 + 128) : "s100");
      }
    }
    const rsrc_t rsDr = MKRS(Dr, D * D * 2), rsDi = MKRS(Di, D * D * 2);
    const rsrc_t rsD3 = lw < 2 ? rsDr : rsDi;
    const unsigned srcD3 = lw < 2 ? srcD + 96 * D * 2 : srcD - 16 * D * 2;
    const unsigned sgw = sgL + wid * 4;
    if (tid < 16) *(lds_u32*)(size_t)(sgL + tid * 4) = 0;
    auto unit = [&](int u, int sl) {
      const unsigned so = u * 128, dst = ldw + sl * STAGE_B;
#pragma unroll
      for (int i = 0; i < 3; ++i) dma16(rsDr, srcD, so + i * (32 * D * 2), dst + i * 4096);
      dma16(rsD3, srcD3, so, dst + (lw < 2 ? 3 * 4096 : 16384 - 2048));
#pragma unroll
      for (int i = 0; i < 3; ++i) dma16(rsDi, srcD, so + (16 + i * 32) * (D * 2), dst + 16384 + 2048 + i * 4096);
    };
#pragma unroll 1
    for (int u = 0; u < 2; ++u) {
      unit(u, u + 1);
      if (u == 0) { WAIT_V(UN + 6); WAIT_L(0); BAR(); WAIT_V(UN); BAR(); }
    }
    mk_dbase();
    WAIT_V(0); sig(sgw, 1);
    wait_sig(sgL, 1);
    rdD(de[0], dbase, 0);
    rdD(de[1], dbase, 1);
    sig(sgw, 2);
    wait_sig(sgC, 1);
#pragma unroll 1
    for (int u = 2; u < 7; ++u) {
      if (u == 5) { wait_sig(sgC, 2); wait_sig(sgL, 2); }
      unit(u, u >= 4 ? u - 4 : u + 1);
      if (u == 5) { WAIT_V(2 * UN); sig(sgw, 3); }
    }
  } else {
    const int cw = wid & 3;
    mk_dbase();
    const rsrc_t rsP = MKRS(ws, PART_BYTES);
    const int wm = cw >> 1, wn = cw & 1;
    const unsigned poff = (ks * 256 + wm * 128 + tb * 4) * PBLK + (((lane >> 5) | ((lane >> 3) & 2)) * 16 + (lane & 15)) * 16;
    const unsigned pfo = lane < 49 ? (((tbl * 2 + ks) * 2 + wn) * 28) * (D * 2) + lane * 128 : 0x7ffffff0u;
    gu64* const epp = (gu64*)(ws + SYNC_OFF) + tb * 32 + 8;
    if (nt < 3) p1_compute<2>(lds0, cw, lane, nt * 4 + wn * 2, poff, rsP, rsP, pfo, epp, ep);
    else p1_compute<1>(lds0, cw, lane, 12 + wn, poff, rsP, MKRS(wm ? Di : Dr, D * D * 2), pfo, epp, ep);
    wait_sig(sgL, 1);
    rdD(de[0], dbase, 0);
    rdD(de[1], dbase, 1);
    sig(sgL + wid * 4, 2);
    SCHED0();
    WAIT_V(0);
    SCHED0();
    ep += EPOCH_STEP;
    if (lane == 0) {
      unsigned old = __hip_atomic_fetch_add((lds_u32*)(size_t)(lds0 + CNT_OFF), 1u, __ATOMIC_RELAXED, __HIP_MEMORY_SCOPE_WORKGROUP);
      if (old == 3) __hip_atomic_store((gu64*)(ws + SYNC_OFF) + tb * 32 + mem, ep, RLX_AGENT);
    }
  }
  gu64* const flags = (gu64*)(ws + SYNC_OFF) + tb * 32;
  wait_sig(sgL, 3);
  unsigned long long f0 = ep;
  if (wid == 4) f0 = __hip_atomic_load(flags + (lane & 7), RLX_AGENT);
  SCHED0();
  rdD(de[2], dbase, 2);
  rdD(de[3], dbase, 3);
  SCHED0();
  if (wid == 4) {
#pragma unroll 1
    for (unsigned spins = 0; spins < (1u << 22); ++spins) {
      if (__all(f0 == ep)) break;
      f0 = __hip_atomic_load(flags + (lane & 7), RLX_AGENT);
    }
  }
  if (wid < 4) WAIT_V(0);
  WAIT_L(0);
  const rsrc_t rsP = MKRS(ws, PART_BYTES);
  unsigned pbase = (im * 128 + tb * 4 + th * 2 + tt) * PBLK + par * 1024 + lane * 16;
  asm volatile("" : "+v"(pbase));
  SCHED0();
  PRIO(3);
  BAR();
  asm volatile("" ::: "memory");
  u32x4 pf[7][2];
  auto ldP = [&](int n) {
#pragma unroll
    for (int s = 0; s < 2; ++s)
      pf[n][s] = __builtin_amdgcn_raw_buffer_load_b128(rsP, pbase, n * 2048 + s * (256 * PBLK), 16);
    SCHED0();
  };
#pragma unroll
  for (int n = 0; n < PB0; ++n) { ldP(n); if (n == 0) PRIO(2); else if (n == 1) PRIO(1); else PRIO(0); }
  SCHED0();
  f32x4 ya[7];
  float one = 1.0f;
  asm volatile("" : "+s"(one));
  int tl = tid;
  asm volatile("" : "+v"(tl));
  const int fr = tl & 15, g = (tl >> 4) & 3;
  unsigned wa[4];
#pragma unroll
  for (int c = 0; c < 4; ++c) {
    wa[c] = lds0 + RED_OFF + (kg * 32 + tt * 16 + fr) * 512 + (g & 1) * 256 + (((2 * c + (g >> 1)) ^ (fr & 7)) << 4);
    asm volatile("" : "+v"(wa[c]));
  }
  unsigned rb = lds0 + RED_OFF + (tl >> 4) * 512 + ((fr ^ ((tl >> 4) & 7)) << 4);
  unsigned yo = fr < 14 ? ((tb * 64 + th * 32 + (tl >> 4)) * D + et * 112 + fr * 8) * 2 : 0x7ffffff0u;
  asm volatile("" : "+v"(rb), "+v"(yo));
  SCHED0();
  PRIO(3);
#pragma unroll
  for (int c = 0; c < 7; ++c) ya[c] = __builtin_amdgcn_mfma_f32_16x16x32_bf16(de[0][c], __builtin_bit_cast(bf16x8, pf[0][0]), f32x4{0.f, 0.f, 0.f, 0.f}, 0, 0, 0);
  bf16x8 ps = __builtin_bit_cast(bf16x8, pf[0][1]);
  SCHED0();
#pragma unroll
  for (int n = 0; n < 7; ++n) {
    if (n == 1) PRIO(2);
    if (n == 2) PRIO(1);
    if (n == 3) PRIO(0);
    if (PB0 + n < 7) ldP(PB0 + n);
    if (n == 6 && tid == 256 && mem == 0) __hip_atomic_store(flags + 8, ep, RLX_AGENT);
    SCHED0();
    bf16x8 pn = ps;
    if (n < 6) pn = add_bf16x8(pf[n + 1][0], pf[n + 1][1], one);
#pragma unroll
    for (int c = 0; c < 7; ++c) ya[c] = __builtin_amdgcn_mfma_f32_16x16x32_bf16(de[n & 3][c], ps, ya[c], 0, 0, 0);
    if (n < 6) {
#pragma unroll
      for (int c = 0; c < 7; ++c) {
        __builtin_amdgcn_sched_group_barrier(0x8, 1, 0);
        __builtin_amdgcn_sched_group_barrier(0x2, 4, 0);
      }
    } else {
#pragma unroll
      for (int c = 0; c < 7; ++c) *(lds_f32x4*)(size_t)(wa[c & 3] + (c >> 2) * 128) = ya[c];
      __builtin_amdgcn_sched_group_barrier(0x8, 3, 0);
#pragma unroll
      for (int c = 0; c < 4; ++c) {
        __builtin_amdgcn_sched_group_barrier(0x200, 1, 0);
        __builtin_amdgcn_sched_group_barrier(0x8, 1, 0);
      }
      __builtin_amdgcn_sched_group_barrier(0x200, 3, 0);
    }
    SCHED0();
    if (n < 3) rdD(de[n], dbase, n + 4);
    SCHED0();
    ps = pn;
  }
  WAIT_L(0);
  BAR();
  {
    f32x4 r[2][4], o[2];
#pragma unroll
    for (int z = 0; z < 2; ++z)
#pragma unroll
      for (int k = 0; k < 4; ++k) r[z][k] = *(const lds_f32x4*)(size_t)(rb + z * 256 + k * (32 * 512));
    SCHED0();
#pragma unroll
    for (int z = 0; z < 2; ++z) o[z] = (r[z][0] + r[z][1]) - (r[z][2] + r[z][3]);
    u32x4 v = {pack2(o[0][0], o[0][1]), pack2(o[0][2], o[0][3]), pack2(o[1][0], o[1][1]), pack2(o[1][2], o[1][3])};
    __builtin_amdgcn_raw_buffer_store_b128(v, MKRS(y, S * D * 2), yo, 0, 16);
  }
}

extern "C" void kernel_launch(void* const* d_in, const int* in_sizes, int n_in, void* d_out, int out_size,
                              void* d_ws, size_t ws_size, hipStream_t stream) {
  const bf16* x = (const bf16*)d_in[0];
  const bf16* Wr = (const bf16*)d_in[1];
  const bf16* Wi = (const bf16*)d_in[2];
  const bf16* Dr = (const bf16*)d_in[3];
  const bf16* Di = (const bf16*)d_in[4];
  (void)hipFuncSetAttribute((const void*)fnet, hipFuncAttributeMaxDynamicSharedMemorySize, LDS_BYTES);
  fnet<<<256, 512, LDS_BYTES, stream>>>(Wr, Wi, x, Dr, Di, (bf16*)d_out, (char*)d_ws);
}
```
